```python
import torch

def _softmax(x, axis=-1):
    return torch.softmax(x, dim=axis)

def _gelu(x):
    return torch.nn.functional.gelu(x, approximate='tanh')

def _silu(x):
    return torch.nn.functional.silu(x)

def _sigmoid(x):
    return torch.sigmoid(x)

def _layernorm(x, axis=-1, eps=1e-05):
    m = x.mean(dim=axis, keepdim=True)
    v = x.var(dim=axis, keepdim=True, unbiased=False)
    return (x - m) / torch.sqrt(v + eps)

def _scatter_add(tgt, idx, src, axis=0):
    return tgt.clone().index_add_(axis, idx, src)

def _segment_sum(src, seg, n):
    out = torch.zeros((n,) + src.shape[1:], dtype=src.dtype)
    return out.index_add_(0, seg, src)

def _pad(x, pw):
    flat = tuple((v for pair in reversed(pw) for v in pair))
    return torch.nn.functional.pad(x, flat)
kernel_name = 'concat_heads__sliding_block__memory_aug_recurrent_mem_tr__prompt'

def setup_inputs():

    _w = lambda a: (lambda t: t.to(torch.bfloat16) if t.is_floating_point() else t)(torch.as_tensor(a))

    def _A_setup():
        B, S, D = (1, 2048, 640)
        return {'x': _w((lambda *s: torch.randn(*s, dtype=torch.bfloat16) * 0.2)(B, S, D)), 'wqkv': _w((lambda *s: torch.randn(*s, dtype=torch.bfloat16) * 0.2)(D, 3 * D)), 'wo': _w((lambda *s: torch.randn(*s, dtype=torch.bfloat16) * 0.05)(D, D))}
    _A_in = _A_setup()

    def _B_setup():
        B, S, Dm = (1, 2048, 640)
        H, Dh = (32, 128)
        M, seg = (8, 32)
        seg = min(seg, S)
        S = S // seg * seg
        return {'x': _w((lambda *s: torch.randn(*s, dtype=torch.bfloat16) * 0.05)(B, S, Dm)), 'mem0': _w((lambda *s: torch.randn(*s, dtype=torch.bfloat16) * 1.6)(B, M, Dm)), 'Wq': _w((lambda *s: torch.randn(*s, dtype=torch.bfloat16) * 0.8)(Dm, H, Dh) * Dm ** (-0.5)), 'Wk': _w((lambda *s: torch.randn(*s, dtype=torch.bfloat16) * 0.8)(Dm, H, Dh) * Dm ** (-0.5)), 'Wv': _w((lambda *s: torch.randn(*s, dtype=torch.bfloat16) * 1.6)(Dm, H, Dh) * Dm ** (-0.5)), 'Wo': _w((lambda *s: torch.randn(*s, dtype=torch.bfloat16) * 1.6)(H, Dh, Dm) * (H * Dh) ** (-0.5)), 'seg_len': seg}
    _B_in = _B_setup()
    return {'A_x': _A_in['x'], 'A_wqkv': _A_in['wqkv'], 'A_wo': _A_in['wo'], 'B_x': _B_in['x'], 'B_mem0': _B_in['mem0'], 'B_Wq': _B_in['Wq'], 'B_Wk': _B_in['Wk'], 'B_Wv': _B_in['Wv'], 'B_Wo': _B_in['Wo'], 'B_seg_len': _B_in['seg_len']}

def reference(A_x, A_wqkv, A_wo, B_x, B_mem0, B_Wq, B_Wk, B_Wv, B_Wo, B_seg_len):

    def _A_ref(x, wqkv, wo):
        B, S, D = x.shape
        H = 32
        Dh = D // H
        W = 256
        qkv = (lambda x, *d: x.permute(*d))((x @ wqkv).reshape(B, S, 3, H, Dh), 2, 0, 3, 1, 4)
        q, k, v = (qkv[0], qkv[1], qkv[2])
        scores = torch.einsum('bhqd,bhkd->bhqk', q, k) * Dh ** (-0.5)
        idx = torch.arange(S)
        mask = (idx[:, None] - idx[None, :] >= 0) & (idx[:, None] - idx[None, :] < W)
        scores = torch.where(mask[None, None], scores, -1000000000.0)
        p = _softmax(scores, axis=-1)
        o = torch.einsum('bhqk,bhkd->bhqd', p, v)
        return (lambda x, *d: x.permute(*d))(o, 0, 2, 1, 3).reshape(B, S, D) @ wo

    def _B_ref(x, mem0, Wq, Wk, Wv, Wo, seg_len):
        B, S, Dm = x.shape
        H, Dh = (Wq.shape[1], Wq.shape[2])
        M = mem0.shape[1]
        scale = Dh ** (-0.5)
        n_seg = S // seg_len
        L = M + seg_len
        idx = torch.arange(L)
        mask = (idx[:, None] < M) | (idx[:, None] >= idx[None, :])
        mem = mem0
        outs = []
        for s in range(n_seg):
            xs = x[:, s * seg_len:(s + 1) * seg_len, :].clone()
            h = torch.concatenate([mem, xs], axis=1)
            q = torch.einsum('btd,dhe->bhte', h, Wq)
            k = torch.einsum('btd,dhe->bhte', h, Wk)
            v = torch.einsum('btd,dhe->bhte', h, Wv)
            scores = torch.einsum('bhqe,bhke->bhqk', q, k) * scale
            scores = torch.where(mask[None, None], scores, -1000000000.0)
            p = _softmax(scores, axis=-1)
            o = torch.einsum('bhqk,bhke->bhqe', p, v)
            o = torch.einsum('bhte,hed->btd', o, Wo)
            mem = o[:, :M, :].clone()
            outs.append(o[:, M:, :])
        return torch.concatenate(outs, axis=1)
    a = _A_ref(A_x, A_wqkv, A_wo)
    b = _B_ref(B_x, B_mem0, B_Wq, B_Wk, B_Wv, B_Wo, B_seg_len)
    return torch.concatenate([a, b], axis=-1)
```

```cpp
#include <hip/hip_runtime.h>
#include <hip/hip_bf16.h>
#include <stdint.h>

#define LAS __attribute__((address_space(3)))
typedef __attribute__((ext_vector_type(8))) __bf16 bf8;
typedef __attribute__((ext_vector_type(4))) __bf16 bf4;
typedef __attribute__((ext_vector_type(2))) __bf16 bf2;
typedef __attribute__((ext_vector_type(16))) __bf16 bf16v;
typedef __attribute__((ext_vector_type(4))) float f4;
typedef __attribute__((ext_vector_type(4))) unsigned u4;
typedef __attribute__((ext_vector_type(2))) unsigned u2;
typedef __attribute__((ext_vector_type(4))) short s4;
typedef LAS char* lptr;
typedef __amdgpu_buffer_rsrc_t rsrc_t;

constexpr unsigned OFF_CTR = 0;
constexpr unsigned OFF_MEM = 65536;
constexpr unsigned MEM_STEP = 10240;
constexpr unsigned OFF_P = OFF_MEM + 66 * MEM_STEP;
constexpr unsigned P_STEP = 20480;
constexpr unsigned INIT_BYTES = OFF_P + 64 * P_STEP;
constexpr unsigned OFF_WVO = 4u << 20;
constexpr unsigned OFF_QT = OFF_WVO + 80u * 327680u;
constexpr unsigned OFF_KT = OFF_QT + 64u * 32u * 8192u;
constexpr unsigned OFF_VOT = OFF_KT + 64u * 32u * 8192u;
constexpr unsigned OFF_VOM = OFF_VOT + 64u * 80u * 16384u;
constexpr unsigned OFF_PT = OFF_VOM + 64u * 80u * 4096u;
constexpr unsigned OFF_QKVA = OFF_PT + 64u * 32u * 2560u;
constexpr unsigned OFF_OA = OFF_QKVA + 2048u * 3840u;
constexpr unsigned WS_USED = OFF_OA + 2048u * 1280u;
static_assert(WS_USED <= (256u << 20), "workspace");
constexpr unsigned SENT = 0xFFFFFFFFu;

struct Params {
  const char* Ax; const char* Awqkv; const char* Awo; const char* Bx; const char* Bmem0;
  const char* Wq; const char* Wk; const char* Wv; const char* Wo; char* out; char* ws;
};

__device__ __forceinline__ f4 mfma16(bf8 a, bf8 b, f4 c) { return __builtin_amdgcn_mfma_f32_16x16x32_bf16(a, b, c, 0, 0, 0); }
__device__ __forceinline__ f4 mfma16k16(bf4 a, bf4 b, f4 c) {
  return __builtin_amdgcn_mfma_f32_16x16x16bf16_1k(__builtin_bit_cast(s4, a), __builtin_bit_cast(s4, b), c, 0, 0, 0);
}
__device__ __forceinline__ f4 smfma(bf8 a, bf16v b, f4 c, int idx) { return __builtin_amdgcn_smfmac_f32_16x16x64_bf16(a, b, c, idx, 0, 0); }
__device__ __forceinline__ unsigned pk2(float a, float b) { bf2 v = {(__bf16)a, (__bf16)b}; return __builtin_bit_cast(unsigned, v); }
__device__ __forceinline__ u4 pk8(f4 a, f4 b) { return u4{pk2(a[0], a[1]), pk2(a[2], a[3]), pk2(b[0], b[1]), pk2(b[2], b[3])}; }
__device__ __forceinline__ bf8 lrd(lptr p) { return *(const LAS bf8*)p; }
__device__ __forceinline__ bf4 lrd64(lptr p) { return *(const LAS bf4*)p; }
__device__ __forceinline__ bf4 ltr(lptr p) { return __builtin_bit_cast(bf4, __builtin_amdgcn_ds_read_tr16_b64_v4i16((LAS s4*)p)); }
__device__ __forceinline__ bf8 cat8(bf4 a, bf4 b) { return __builtin_shufflevector(a, b, 0, 1, 2, 3, 4, 5, 6, 7); }
__device__ __forceinline__ bf8 asbf8(u4 v) { return __builtin_bit_cast(bf8, v); }
__device__ __forceinline__ rsrc_t mk_rsrc(const void* p, unsigned bytes) { return __builtin_amdgcn_make_buffer_rsrc((void*)p, 0, bytes, 0x00020000); }
template <bool SC1>
__device__ __forceinline__ void blds(rsrc_t r, unsigned voff, unsigned soff, unsigned lds) {
  if (SC1) asm volatile("s_mov_b32 m0, %0\n\ts_nop 0\n\tbuffer_load_dwordx4 %1, %2, %3 offen sc1 lds" ::"s"(lds), "v"(voff), "s"(r), "s"(soff) : "memory");
  else asm volatile("s_mov_b32 m0, %0\n\ts_nop 0\n\tbuffer_load_dwordx4 %1, %2, %3 offen lds" ::"s"(lds), "v"(voff), "s"(r), "s"(soff) : "memory");
}
__device__ __forceinline__ u4 gld128(rsrc_t r, unsigned voff, unsigned soff) { return __builtin_amdgcn_raw_buffer_load_b128(r, voff, soff, 16); }
__device__ __forceinline__ void gst128(rsrc_t r, u4 v, unsigned voff, unsigned soff) { __builtin_amdgcn_raw_buffer_store_b128(v, r, voff, soff, 16); }
__device__ __forceinline__ void gst64(rsrc_t r, u2 v, unsigned voff, unsigned soff) { __builtin_amdgcn_raw_buffer_store_b64(v, r, voff, soff, 16); }
#define WAITV(n) asm volatile("s_waitcnt vmcnt(" #n ")" ::: "memory")
#define WAITL(n) asm volatile("s_waitcnt lgkmcnt(" #n ")" ::: "memory")
#define BAR() do { asm volatile("" ::: "memory"); __builtin_amdgcn_s_barrier(); asm volatile("" ::: "memory"); } while (0)
#define SCHEDB() __builtin_amdgcn_sched_barrier(0)
__device__ __forceinline__ unsigned umin32(unsigned a, unsigned b) { return a < b ? a : b; }
__device__ __forceinline__ unsigned umax3(unsigned a, unsigned b, unsigned c) { unsigned m = a > b ? a : b; return m > c ? m : c; }
__device__ __forceinline__ unsigned nos(unsigned x) { return umin32(x, 0xFFFFFFFEu); }
__device__ __forceinline__ void swap32(float& a, float& b) {
  auto r = __builtin_amdgcn_permlane32_swap(__builtin_bit_cast(unsigned, a), __builtin_bit_cast(unsigned, b), false, false);
  const unsigned r0 = r[0], r1 = r[1];
  a = __builtin_bit_cast(float, r0); b = __builtin_bit_cast(float, r1);
}
__device__ __forceinline__ void swap16u(unsigned& a, unsigned& b) {
  auto r = __builtin_amdgcn_permlane16_swap(a, b, false, false);
  a = r[0]; b = r[1];
}
__device__ __forceinline__ float xmax4(float x) {
  unsigned a = __builtin_bit_cast(unsigned, x), b = a;
  swap16u(a, b);
  float y = fmaxf(__builtin_bit_cast(float, a), __builtin_bit_cast(float, b)), z = y;
  swap32(y, z);
  return fmaxf(y, z);
}
__device__ __forceinline__ float xsum4(float x) {
  unsigned a = __builtin_bit_cast(unsigned, x), b = a;
  swap16u(a, b);
  float y = __builtin_bit_cast(float, a) + __builtin_bit_cast(float, b), z = y;
  swap32(y, z);
  return y + z;
}
constexpr float NEG_INF = -__builtin_huge_valf();

constexpr int STG = 32768;
constexpr int LDS_V = 3 * STG;

template <int NB, bool XSC1, class After>
__device__ __forceinline__ void gemm_p1(lptr sm, rsrc_t rx, unsigned xrow0, rsrc_t r0, rsrc_t r1, rsrc_t r2, unsigned wcol_bytes, unsigned ldw2,
                                        f4 (&acc)[4][2 * NB], After after) {
  const int l = threadIdx.x & 63, w = __builtin_amdgcn_readfirstlane(threadIdx.x >> 6);
  const int tg = w & 1, cg = w >> 1;
  const unsigned smb = (unsigned)(size_t)sm;
  const int rho = l >> 2;
  const unsigned vx = (xrow0 + 32 * (w >> 1) + 8 * (rho >> 2) + 4 * (w & 1) + (rho & 3)) * 1280u + (((l & 3) ^ (((rho >> 3) & 1) << 1)) * 16);
  const int grow = 8 * (w & 3) + ((l >> 2) & 7);
  const unsigned vw = grow * ldw2 + (64 * (w >> 2) + 32 * (l >> 5) + 8 * ((l & 3) ^ ((grow >> 2) & 3))) * 2 + wcol_bytes;
  auto stage = [&](int s) {
    const unsigned b = smb + (s % 3) * STG;
    blds<XSC1>(rx, vx, s * 64, b + w * 1024);
    const unsigned ko = (unsigned)s * 32u * ldw2;
    const unsigned bw = b + 8192 + (w >> 2) * 4096 + (w & 3) * 1024;
    blds<false>(r0, vw, ko, bw);
    blds<false>(r1, vw, ko, bw + 8192);
    if (NB == 3) blds<false>(r2, vw, ko, bw + 16384);
  };
  const int g = l >> 4, q = (l & 15) >> 2, p = l & 3;
  const int ax = (4 * tg) * 1024 + (l & 15) * 64 + ((g ^ ((((l & 15) >> 3) & 1) << 1)) * 16);
  int aw[2][2];
#pragma unroll
  for (int t = 0; t < 2; ++t)
#pragma unroll
    for (int b = 0; b < 2; ++b)
      aw[t][b] = 8192 + 4096 * (cg >> 1) + 512 * (cg & 1) + 1024 * g + 64 * (4 * t + q) + 16 * ((2 * b + (p >> 1)) ^ ((2 * g + t) & 3)) + 8 * (p & 1);
  struct Fr { bf8 x[4]; bf8 w[2 * NB]; };
  auto rd = [&](Fr& f, int s) {
    lptr b = sm + (s % 3) * STG;
#pragma unroll
    for (int i = 0; i < 4; ++i) f.x[i] = lrd(b + ax + i * 1024);
#pragma unroll
    for (int j = 0; j < 2 * NB; ++j) f.w[j] = cat8(ltr(b + aw[0][j & 1] + 8192 * (j >> 1)), ltr(b + aw[1][j & 1] + 8192 * (j >> 1)));
  };
#pragma unroll
  for (int i = 0; i < 4; ++i)
#pragma unroll
    for (int j = 0; j < 2 * NB; ++j) acc[i][j] = f4{0, 0, 0, 0};
  stage(0); stage(1); stage(2);
  if (NB == 3) WAITV(8); else WAITV(6);
  BAR();
  Fr f[2];
  rd(f[0], 0);
#pragma unroll
  for (int t = 0; t < 20; ++t) {
    if (t + 1 < 20) {
      if (NB == 3) { if (t + 2 < 20 || After::N > 0) WAITV(4); else WAITV(0); }
      else { if (t + 2 < 20) WAITV(3); else WAITV(0); }
      WAITL(0); BAR();
      if (t + 3 < 20) stage(t + 3); else after.stage(t + 3);
      rd(f[(t + 1) & 1], t + 1);
    }
#pragma unroll
    for (int j = 0; j < 2 * NB; ++j)
#pragma unroll
      for (int i = 0; i < 4; ++i) acc[i][j] = mfma16(f[t & 1].w[j], f[t & 1].x[i], acc[i][j]);
    if (t + 1 < 20) {
#pragma unroll
      for (int k = 0; k < 4 + 4 * NB; ++k) { __builtin_amdgcn_sched_group_barrier(0x008, 1, 0); __builtin_amdgcn_sched_group_barrier(0x100, 1, 0); }
    }
    SCHEDB();
  }
}
struct NoAfter { static constexpr int N = 0; __device__ __forceinline__ void stage(int) const {} };

template <bool PERM>
struct WoStager {
  static constexpr int N = 5;
  rsrc_t r; unsigned voff, smb; int s0;
  __device__ __forceinline__ void init(rsrc_t rwo, lptr sm, int h, int s0_) {
    const int l = threadIdx.x & 63, w = __builtin_amdgcn_readfirstlane(threadIdx.x >> 6);
    const int lam = 8 * (w & 3) + ((l >> 2) & 7);
    const int e = PERM ? 16 * ((lam >> 2) & 1) + 4 * (lam >> 3) + (lam & 3) : lam;
    r = rwo; smb = (unsigned)(size_t)sm; s0 = s0_;
    voff = (h * 128 + e) * 1280u + (64 * (w >> 2) + 32 * (l >> 5) + 8 * ((l & 3) ^ ((lam >> 2) & 3))) * 2;
  }
  __device__ __forceinline__ void stage(int s) const {
    const int pi = s - s0;
    if (pi >= 5) return;
    const int w = __builtin_amdgcn_readfirstlane(threadIdx.x >> 6);
    const unsigned b = smb + (s % 3) * STG + (w >> 2) * 4096 + (w & 3) * 1024;
#pragma unroll
    for (int u = 0; u < 4; ++u) blds<false>(r, voff, u * 32 * 1280 + pi * 256, b + u * 8192);
  }
};
template <class St, class Epi>
__device__ __forceinline__ void gemm_p2(lptr sm, const St& st, int s0, const bf8 (&a)[4][4], Epi epi) {
  const int l = threadIdx.x & 63, w = __builtin_amdgcn_readfirstlane(threadIdx.x >> 6);
  const int cg = w >> 1;
  const int g = l >> 4, q = (l & 15) >> 2, p = l & 3;
  int aw[2][2];
#pragma unroll
  for (int t = 0; t < 2; ++t)
#pragma unroll
    for (int b = 0; b < 2; ++b)
      aw[t][b] = 4096 * (cg >> 1) + 512 * (cg & 1) + 1024 * g + 64 * (4 * t + q) + 16 * ((2 * b + (p >> 1)) ^ ((2 * g + t) & 3)) + 8 * (p & 1);
#pragma unroll
  for (int pi = 0; pi < 5; ++pi) {
    if (pi == 1) WAITV(20); else if (pi < 4) WAITV(16); else WAITV(12);
    BAR();
    lptr b = sm + ((s0 + pi) % 3) * STG;
    bf8 wf[4][2];
#pragma unroll
    for (int m = 0; m < 4; ++m)
#pragma unroll
      for (int bb = 0; bb < 2; ++bb) wf[m][bb] = cat8(ltr(b + m * 8192 + aw[0][bb]), ltr(b + m * 8192 + aw[1][bb]));
    f4 acc[4][2];
#pragma unroll
    for (int i = 0; i < 4; ++i) { acc[i][0] = f4{0, 0, 0, 0}; acc[i][1] = f4{0, 0, 0, 0}; }
#pragma unroll
    for (int m = 0; m < 4; ++m)
#pragma unroll
      for (int bb = 0; bb < 2; ++bb)
#pragma unroll
        for (int i = 0; i < 4; ++i) acc[i][bb] = mfma16(a[i][m], wf[m][bb], acc[i][bb]);
    WAITL(0); BAR();
    st.stage(s0 + pi + 3);
    epi(pi, acc);
  }
}

__device__ __forceinline__ void task_bproj(const Params& P, lptr sm, int blk, int h) {
  const int l = threadIdx.x & 63, w = __builtin_amdgcn_readfirstlane(threadIdx.x >> 6);
  const int tg = w & 1, cg = w >> 1;
  const rsrc_t rx = mk_rsrc(P.Bx, 2048 * 1280), rq = mk_rsrc(P.Wq, 640 * 8192), rk = mk_rsrc(P.Wk, 640 * 8192), rv = mk_rsrc(P.Wv, 640 * 8192);
  const rsrc_t rwo = mk_rsrc(P.Wo, 4096 * 1280), rws = mk_rsrc(P.ws, WS_USED);
  WoStager<true> st; st.init(rwo, sm, h, 20);
  f4 acc[4][6];
  gemm_p1<3, false>(sm, rx, 128 * blk, rq, rk, rv, h * 256, 8192, acc, st);
#pragma unroll
  for (int jj = 0; jj < 3; ++jj) {
    const int ty = jj, m = cg;
#pragma unroll
    for (int i = 0; i < 4; ++i) {
      const u4 v = pk8(acc[i][2 * jj], acc[i][2 * jj + 1]);
      const int tt = 4 * tg + i;
      if (ty == 2) *(LAS u4*)(sm + LDS_V + (tt * 4 + m) * 1024 + l * 16) = v;
      else gst128(rws, v, l * 16, (ty ? OFF_KT : OFF_QT) + (((4 * blk + (tt >> 1)) * 32 + h) * 8 + (tt & 1) * 4 + m) * 1024);
    }
  }
  WAITL(0); BAR();
  st.stage(22);
  bf8 a[4][4];
#pragma unroll
  for (int i = 0; i < 4; ++i)
#pragma unroll
    for (int m = 0; m < 4; ++m) a[i][m] = lrd(sm + LDS_V + ((4 * tg + i) * 4 + m) * 1024 + l * 16);
  const unsigned vo = (l >> 4) * 256 + (h & 1) * 128 + (l & 7) * 16 + ((l >> 3) & 1) * 16384;
  gemm_p2(sm, st, 20, a, [&](int pi, f4 (&ac)[4][2]) {
#pragma unroll
    for (int sg = 0; sg < 2; ++sg)
#pragma unroll
      for (int bb = 0; bb < 2; ++bb) {
        const int seg = 4 * blk + 2 * tg + sg, c0 = 16 * pi + 4 * cg + 2 * bb;
        gst128(rws, pk8(ac[2 * sg][bb], ac[2 * sg + 1][bb]), vo, OFF_VOT + ((seg * 80 + c0) * 16 + (h >> 1)) * 1024);
      }
  });
  WAITV(0); BAR();
}

__device__ __forceinline__ void task_fold(const Params& P, lptr sm, int h, int db) {
  const int l = threadIdx.x & 63, w = __builtin_amdgcn_readfirstlane(threadIdx.x >> 6);
  const int tg = w & 1, cg = w >> 1;
  const rsrc_t rv = mk_rsrc(P.Wv, 640 * 8192), rwo = mk_rsrc(P.Wo, 4096 * 1280), rws = mk_rsrc(P.ws, WS_USED);
  WoStager<false> st; st.init(rwo, sm, h, 0);
  st.stage(0); st.stage(1); st.stage(2);
  const int kb = 2 * db + tg;
  bf8 a[4][4];
  {
    const int rho = l & 15, lg = rho >> 2, r = rho & 3;
#pragma unroll
    for (int t = 0; t < 4; ++t) {
      const int din = 64 * kb + 16 * (2 * (t >> 1) + (lg >> 1)) + 8 * ((r >> 1) & 1) + 4 * (lg & 1) + 2 * (t & 1) + (r & 1);
#pragma unroll
      for (int m = 0; m < 4; ++m)
        a[t][m] = asbf8(__builtin_amdgcn_raw_buffer_load_b128(rv, din * 8192 + h * 256 + m * 64 + (l >> 4) * 16, 0, 0));
    }
  }
  WAITV(0);
  const unsigned vo = (l >> 4) * 256 + (h & 1) * 128 + (l & 7) * 16 + ((l >> 3) & 1) * 327680;
  gemm_p2(sm, st, 0, a, [&](int pi, f4 (&ac)[4][2]) {
#pragma unroll
    for (int bb = 0; bb < 2; ++bb) {
      const int c0 = 16 * pi + 4 * cg + 2 * bb;
#pragma unroll
      for (int part = 0; part < 2; ++part)
        gst128(rws, pk8(ac[2 * part][bb], ac[2 * part + 1][bb]), vo, OFF_WVO + c0 * 327680 + ((h >> 1) * 10 + kb) * 2048 + part * 1024);
    }
  });
  WAITV(0); BAR();
}

__device__ __forceinline__ u4 rowpack(f4 t0, f4 t1) {
  unsigned a0 = pk2(t0[0], t0[1]), a1 = pk2(t0[2], t0[3]), b0 = pk2(t1[0], t1[1]), b1 = pk2(t1[2], t1[3]);
  swap16u(a0, b0); swap16u(a1, b1);
  return u4{a0, a1, b0, b1};
}
__device__ __forceinline__ int rowpack_col(int lg) { return 16 * (lg & 1) + 8 * (lg >> 1); }

template <int NB, bool XSC1>
__device__ __forceinline__ void task_gemm_rows(lptr sm, rsrc_t rx, int blk, const char* wbase, unsigned wbytes, unsigned col0, unsigned ldw2, rsrc_t ro, unsigned obase, unsigned ldo2) {
  const int l = threadIdx.x & 63, w = __builtin_amdgcn_readfirstlane(threadIdx.x >> 6);
  const int tg = w & 1, cg = w >> 1;
  f4 acc[4][2 * NB];
  gemm_p1<NB, XSC1>(sm, rx, 128 * blk, mk_rsrc(wbase, wbytes), mk_rsrc(wbase + 256, wbytes - 256), mk_rsrc(wbase + 512, wbytes - 512), col0 * 2, ldw2, acc, NoAfter{});
#pragma unroll
  for (int jj = 0; jj < NB; ++jj) {
    const int pr = 4 * jj + cg;
#pragma unroll
    for (int i = 0; i < 4; ++i) {
      const int tt = 4 * tg + i, rho = l & 15;
      const int tok = 128 * blk + 32 * (tt >> 1) + 8 * (rho >> 2) + 4 * (tt & 1) + (rho & 3);
      gst128(ro, rowpack(acc[i][2 * jj], acc[i][2 * jj + 1]), tok * ldo2 + rowpack_col(l >> 4) * 2, obase + (col0 + 32 * pr) * 2);
    }
  }
  WAITV(0); BAR();
}

constexpr int A2_K = 0, A2_V = 61440, A2_ONE = 122880;
template <bool EDGE>
__device__ __forceinline__ void task_attn(const Params& P, lptr sm, int qb, int hg) {
  const int l = threadIdx.x & 63, w = __builtin_amdgcn_readfirstlane(threadIdx.x >> 6);
  const unsigned smb = (unsigned)(size_t)sm;
  const rsrc_t rws = mk_rsrc(P.ws, WS_USED);
  const int q0 = 128 * qb, key0 = q0 - 256;
#pragma unroll
  for (int y = 0; y < 15; ++y) {
    const int pc = w + 8 * y;
    const int u = (pc % 60) * 64 + l, row = u / 10, un = u - row * 10;
    int tok = key0 + row; if (EDGE) tok = tok < 0 ? 0 : tok;
    blds<true>(rws, tok * 3840 + un * 16 + (pc < 60 ? 1280 : 2560) + hg * 160, OFF_QKVA, smb + pc * 1024);
  }
  if (threadIdx.x == 0) *(LAS u2*)(sm + A2_ONE) = u2{0x00003F80u, 0u};
  const int col = l & 15, lg = l >> 4;
  const int qw = q0 + 16 * w;
  bf8 qf[4];
#pragma unroll
  for (int hh = 0; hh < 4; ++hh) {
    const unsigned base = (qw + col) * 3840 + (4 * hg + hh) * 40;
    const int c0 = lg < 2 ? 2 * lg : 4, c1 = lg < 2 ? 2 * lg + 1 : 4;
    u2 a = __builtin_amdgcn_raw_buffer_load_b64(rws, base + c0 * 8, OFF_QKVA, 16);
    u2 b = __builtin_amdgcn_raw_buffer_load_b64(rws, base + c1 * 8, OFF_QKVA, 16);
    if (lg == 3) a = u2{0, 0};
    if (lg >= 2) b = u2{0, 0};
    qf[hh] = asbf8(u4{a[0], a[1], b[0], b[1]});
  }
  WAITV(0); WAITL(0); BAR();
  const float cs = 0.22360679774997896f * 1.4426950408889634f;
  const int q = (l & 15) >> 2, p = l & 3;
#pragma unroll 1
  for (int hh = 0; hh < 4; ++hh) {
    bf8 qv = hh == 0 ? qf[0] : hh == 1 ? qf[1] : hh == 2 ? qf[2] : qf[3];
    lptr kb = sm + A2_K + (16 * w + col) * 160 + hh * 40;
    const int kc0 = (lg < 2 ? 2 * lg : lg == 2 ? 4 : 0) * 8, kc1 = (lg < 2 ? 2 * lg + 1 : lg == 2 ? 3 : 1) * 8;
    f4 s[17];
#pragma unroll
    for (int kt = 0; kt < 17; ++kt) s[kt] = mfma16(cat8(lrd64(kb + kt * 2560 + kc0), lrd64(kb + kt * 2560 + kc1)), qv, f4{0, 0, 0, 0});
#pragma unroll
    for (int r = 0; r < 4; ++r) {
      if (!(4 * lg + r > col)) s[0][r] = NEG_INF;
      if (!(col >= 4 * lg + r)) s[16][r] = NEG_INF;
    }
    if (EDGE) {
      const int kt0 = 16 - (qw >> 4);
#pragma unroll
      for (int kt = 0; kt < 16; ++kt) if (kt < kt0) s[kt] = f4{NEG_INF, NEG_INF, NEG_INF, NEG_INF};
    }
    float m = s[16][0];
#pragma unroll
    for (int kt = 0; kt < 17; ++kt) m = fmaxf(fmaxf(m, fmaxf(s[kt][0], s[kt][1])), fmaxf(s[kt][2], s[kt][3]));
    m = xmax4(m);
    const float mc = m * cs;
    bf4 pb[17];
#pragma unroll
    for (int kt = 0; kt < 17; ++kt) {
      float e[4];
#pragma unroll
      for (int r = 0; r < 4; ++r) e[r] = __builtin_amdgcn_exp2f(__builtin_fmaf(s[kt][r], cs, -mc));
      pb[kt] = __builtin_bit_cast(bf4, u2{pk2(e[0], e[1]), pk2(e[2], e[3])});
    }
    lptr vb0 = sm + A2_V + (16 * w + 4 * lg + q) * 160 + hh * 40 + p * 8;
    lptr vb1 = p == 0 ? sm + A2_V + (16 * w + 4 * lg + q) * 160 + hh * 40 + 32 : sm + A2_ONE;
    const int st1 = p == 0 ? 2560 : 0;
    f4 o0 = {0, 0, 0, 0}, o1 = {0, 0, 0, 0};
#pragma unroll
    for (int m2 = 0; m2 < 8; ++m2) {
      const bf8 pp = cat8(pb[2 * m2], pb[2 * m2 + 1]);
      o0 = mfma16(cat8(ltr(vb0 + (2 * m2) * 2560), ltr(vb0 + (2 * m2 + 1) * 2560)), pp, o0);
      o1 = mfma16(cat8(ltr(vb1 + (2 * m2) * st1), ltr(vb1 + (2 * m2 + 1) * st1)), pp, o1);
    }
    o0 = mfma16k16(ltr(vb0 + 16 * 2560), pb[16], o0);
    o1 = mfma16k16(ltr(vb1 + 16 * st1), pb[16], o1);
    const float lsum = __shfl(o1[0], 16 + col);
    const float inv = 1.0f / lsum;
    const unsigned ob = (qw + col) * 1280 + (4 * hg + hh) * 40;
    gst64(rws, u2{pk2(o0[0] * inv, o0[1] * inv), pk2(o0[2] * inv, o0[3] * inv)}, ob + lg * 8, OFF_OA);
    if (lg == 0) gst64(rws, u2{pk2(o1[0] * inv, o1[1] * inv), pk2(o1[2] * inv, o1[3] * inv)}, ob + 32, OFF_OA);
  }
  WAITV(0); BAR();
}

__device__ __forceinline__ void task_tout(const Params& P, lptr sm, int seg, int cgp) {
  const int l = threadIdx.x & 63, w = __builtin_amdgcn_readfirstlane(threadIdx.x >> 6);
  const unsigned smb = (unsigned)(size_t)sm;
  const rsrc_t rws = mk_rsrc(P.ws, WS_USED), ro = mk_rsrc(P.out, 2048 * 2560);
#pragma unroll
  for (int y = 0; y < 10; ++y) blds<true>(rws, l * 16, OFF_PT + seg * 81920 + (w + 8 * y) * 1024, smb + (w + 8 * y) * 1024);
  const int col = l & 15, lg = l >> 4;
  const int c0 = 16 * cgp + 2 * w;
  const unsigned vt = (col >> 3) * 16384 + lg * 256 + (col & 7) * 16, st = OFF_VOT + (seg * 80 + c0) * 16384;
  const unsigned vm = (col >> 3) * 4096 + lg * 256 + (col & 7) * 16, smo = OFF_VOM + (seg * 80 + c0) * 4096;
  u4 a[40];
#pragma unroll
  for (int k = 0; k < 16; ++k) a[k] = gld128(rws, vt, st + (k >> 1) * 1024 + (k & 1) * 128);
  WAITV(16); BAR();
  lptr bt = sm + (col >> 3) * 512 + lg * 128 + (col & 7) * 16;
  lptr bm = sm + lg * 2560 + 2048 + col * 16;
  f4 acc[2] = {f4{0, 0, 0, 0}, f4{0, 0, 0, 0}};
#pragma unroll
  for (int k = 0; k < 40; ++k) {
    const int kn = k + 16;
    if (kn < 32) a[kn] = gld128(rws, vt, st + (kn >> 1) * 1024 + (kn & 1) * 128);
    else if (kn < 40) a[kn] = gld128(rws, vm, smo + ((kn - 32) >> 1) * 1024 + ((kn - 32) & 1) * 128);
#pragma unroll
    for (int tt = 0; tt < 2; ++tt) {
      const bf8 b = k < 32 ? lrd(bt + k * 2560 + tt * 1024) : lrd(bm + (k - 32) * 4 * 2560 + tt * 256);
      acc[tt] = mfma16(asbf8(a[k]), b, acc[tt]);
    }
  }
  unsigned a0 = pk2(acc[0][0], acc[0][1]), a1 = pk2(acc[0][2], acc[0][3]), b0 = pk2(acc[1][0], acc[1][1]), b1 = pk2(acc[1][2], acc[1][3]);
  swap16u(a0, b0); swap16u(a1, b1);
  const int tok = 32 * seg + 16 * (lg & 1) + col;
  gst128(ro, u4{a0, a1, b0, b1}, tok * 2560 + 1280 + (128 * cgp + 16 * w + 8 * (lg >> 1)) * 2, 0);
  WAITV(0); BAR();
}

constexpr int NSTEP = 64;
constexpr unsigned SPIN_MAX = 1u << 14;
constexpr int XL_MEM = 0, XL_T = 10240, XL_KQ = 16384, XL_W = 49152;
constexpr float CS_B = 0.08838834764831845f * 1.4426950408889634f;

template <class WF>
__device__ __forceinline__ void gather_project(lptr sm, unsigned memoff, rsrc_t rws, int s, int l, int w, int idx, WF wf, f4& d0, f4& d1, unsigned& budget) {
  const unsigned smb = (unsigned)(size_t)sm + memoff;
  for (;;) {
    blds<true>(rws, l * 16, OFF_MEM + s * MEM_STEP + w * 1024, smb + w * 1024);
    if (w < 2) blds<true>(rws, l * 16, OFF_MEM + s * MEM_STEP + (8 + w) * 1024, smb + (8 + w) * 1024);
    WAITV(0); BAR();
    d0 = f4{0, 0, 0, 0}; d1 = f4{0, 0, 0, 0};
    unsigned mx = 0;
#pragma unroll
    for (int kb = 0; kb < 10; ++kb) {
      const u4 a = *(const LAS u4*)(sm + memoff + kb * 1024 + l * 16);
      mx = umax3(umax3(mx, a[0], a[1]), a[2], a[3]);
      d0 = smfma(asbf8(a), wf(0, kb), d0, idx);
      d1 = smfma(asbf8(a), wf(1, kb), d1, idx);
    }
    if (!__any(mx == SENT) || budget == 0) break;
    --budget;
    BAR();
  }
}

__device__ __forceinline__ void softmax40(f4 t0, f4 t1, f4 mm, int lg, u4& ptok, u2& pmem) {
  float m = fmaxf(fmaxf(fmaxf(t0[0], t0[1]), fmaxf(t0[2], t0[3])), fmaxf(fmaxf(t1[0], t1[1]), fmaxf(t1[2], t1[3])));
  if (lg < 2) mm = f4{NEG_INF, NEG_INF, NEG_INF, NEG_INF};
  m = fmaxf(m, fmaxf(fmaxf(mm[0], mm[1]), fmaxf(mm[2], mm[3])));
  m = xmax4(m);
  const float mc = m * CS_B;
  float sum = 0;
#pragma unroll
  for (int r = 0; r < 4; ++r) {
    t0[r] = __builtin_amdgcn_exp2f(__builtin_fmaf(t0[r], CS_B, -mc));
    t1[r] = __builtin_amdgcn_exp2f(__builtin_fmaf(t1[r], CS_B, -mc));
    mm[r] = __builtin_amdgcn_exp2f(__builtin_fmaf(mm[r], CS_B, -mc));
    sum += t0[r] + t1[r] + mm[r];
  }
  const float inv = 1.0f / xsum4(sum);
  ptok = u4{nos(pk2(t0[0] * inv, t0[1] * inv)), nos(pk2(t0[2] * inv, t0[3] * inv)), nos(pk2(t1[0] * inv, t1[1] * inv)), nos(pk2(t1[2] * inv, t1[3] * inv))};
  pmem = u2{nos(pk2(mm[0] * inv, mm[1] * inv)), nos(pk2(mm[2] * inv, mm[3] * inv))};
}

__device__ __forceinline__ void role_x(const Params& P, lptr sm, int h) {
  const int l = threadIdx.x & 63, w = __builtin_amdgcn_readfirstlane(threadIdx.x >> 6);
  const unsigned smb = (unsigned)(size_t)sm;
  const rsrc_t rws = mk_rsrc(P.ws, WS_USED), rq = mk_rsrc(P.Wq, 640 * 8192), rk = mk_rsrc(P.Wk, 640 * 8192);
  const int g = l >> 4, q = (l & 15) >> 2, p = l & 3, col = l & 15;
  bf16v wf[2][10];
  {
    auto rho = [](int row) { return ((row >> 3) & 1) * 4 + ((row >> 2) & 1) * 2 + (row & 1); };
    auto stage = [&](int kb) {
#pragma unroll
      for (int y = 0; y < 2; ++y) {
        const int pc = w + 8 * y, row = 4 * pc + (l >> 4), un = l & 15;
        const unsigned vo = row * 8192 + h * 256 + (((un >> 1) ^ rho(row)) * 32) + (un & 1) * 16;
        blds<false>(rq, vo, kb * 64 * 8192, smb + XL_W + (kb & 1) * 32768 + pc * 1024);
        blds<false>(rk, vo, kb * 64 * 8192, smb + XL_W + (kb & 1) * 32768 + 16384 + pc * 1024);
      }
    };
    stage(0);
#pragma unroll
    for (int kb = 0; kb < 10; ++kb) {
      WAITV(0); BAR();
      if (kb + 1 < 10) stage(kb + 1);
#pragma unroll
      for (int ty = 0; ty < 2; ++ty) {
        bf4 v[4];
#pragma unroll
        for (int u = 0; u < 4; ++u) {
          const int row = 16 * (2 * (u >> 1) + (g >> 1)) + 8 * ((q >> 1) & 1) + 4 * (g & 1) + 2 * (u & 1) + (q & 1);
          v[u] = ltr(sm + XL_W + (kb & 1) * 32768 + ty * 16384 + row * 256 + ((w ^ rho(row)) * 32) + p * 8);
        }
        const bf8 lo = cat8(v[0], v[1]), hi = cat8(v[2], v[3]);
        wf[ty][kb] = __builtin_shufflevector(lo, hi, 0, 1, 2, 3, 4, 5, 6, 7, 8, 9, 10, 11, 12, 13, 14, 15);
      }
      WAITL(0);
    }
  }
  const int idx = (l & 8) ? 0xEEEE : 0x4444;
  auto prefetch = [&](int s) {
    if (w >= 4) {
#pragma unroll
      for (int y = 0; y < 4; ++y) {
        const int pc = (w - 4) * 4 + y;
        blds<true>(rws, l * 16, (pc < 8 ? OFF_KT : OFF_QT) + (s * 32 + h) * 8192 + (pc & 7) * 1024, smb + XL_KQ + (s & 1) * 16384 + pc * 1024);
      }
    }
  };
  prefetch(0);
  unsigned budget = SPIN_MAX;
  for (int s = 0; s < NSTEP; ++s) {
    f4 dq, dk;
    gather_project(sm, XL_MEM, rws, s, l, w, idx, [&](int t, int kb) { return wf[t][kb]; }, dq, dk, budget);
#pragma unroll
    for (int r = 0; r < 4; ++r) { float a = dq[r], b = dk[r]; swap32(a, b); dq[r] = a + b; }
    *(LAS u2*)(sm + XL_T + (16 * w + col) * 32 + (l >> 5) * 16 + (g & 1) * 8) = u2{pk2(dq[0], dq[1]), pk2(dq[2], dq[3])};
    WAITL(0); BAR();
    if (s + 1 < NSTEP) prefetch(s + 1);
    if (w < 3) {
      lptr kq = sm + XL_KQ + (s & 1) * 16384;
      f4 mm = {0, 0, 0, 0}, t0 = {0, 0, 0, 0}, t1 = {0, 0, 0, 0};
#pragma unroll
      for (int m = 0; m < 4; ++m) {
        lptr tb = sm + XL_T + (32 * m + 4 * g + q) * 32 + p * 8;
        const bf8 F = cat8(ltr(tb), ltr(tb + 512));
        const bf8 B = w == 0 ? F : lrd(kq + 8192 + ((w - 1) * 4 + m) * 1024 + l * 16);
        mm = mfma16(F, B, mm);
        t0 = mfma16(lrd(kq + m * 1024 + l * 16), B, t0);
        t1 = mfma16(lrd(kq + (4 + m) * 1024 + l * 16), B, t1);
      }
      u4 pt; u2 pm;
      if (w == 0) {
        softmax40(t0, t1, mm, g, pt, pm);
        if (col < 8 && s + 1 < NSTEP) {
          gst128(rws, pt, (g * 8 + col) * 16, OFF_P + s * P_STEP + h * 640);
          if (g >= 2) gst64(rws, pm, 512 + col * 16 + (g - 2) * 8, OFF_P + s * P_STEP + h * 640);
        }
      } else {
        const int tq = 8 * (col >> 2) + (col & 3) + 4 * (w - 1);
#pragma unroll
        for (int r = 0; r < 4; ++r) {
          if (8 * g + r > tq) t0[r] = NEG_INF;
          if (8 * g + 4 + r > tq) t1[r] = NEG_INF;
        }
        softmax40(t0, t1, mm, g, pt, pm);
        const unsigned so = OFF_PT + (s * 32 + h) * 2560;
        gst128(rws, pt, (((tq >> 3) * 4 + g) * 8 + (tq & 7)) * 16, so);
        if (g >= 2) gst64(rws, pm, 2048 + tq * 16 + (g - 2) * 8, so);
      }
    }
  }
  WAITV(0);
}

constexpr int ZL_MEM = 0, ZL_VOM = 10240, ZL_VOT = 16384, ZL_W = 49152;
__device__ __forceinline__ void z_common(lptr sm, rsrc_t rws, int c, int s, int l, int w, f4 d0, f4 d1) {
#pragma unroll
  for (int r = 0; r < 4; ++r) { float a = d0[r], b = d1[r]; swap32(a, b); d0[r] = a + b; }
  const int kg = 2 * (l >> 5) + ((l >> 3) & 1);
  *(LAS u2*)(sm + ZL_VOM + (w >> 1) * 1024 + (kg * 16 + (w & 1) * 8 + (l & 7)) * 16 + ((l >> 4) & 1) * 8) = u2{pk2(d0[0], d0[1]), pk2(d0[2], d0[3])};
  WAITL(0); BAR();
}
__device__ __forceinline__ void role_z(const Params& P, lptr sm, int c) {
  const int l = threadIdx.x & 63, w = __builtin_amdgcn_readfirstlane(threadIdx.x >> 6);
  const unsigned smb = (unsigned)(size_t)sm;
  const rsrc_t rws = mk_rsrc(P.ws, WS_USED);
  const int idx = (l & 8) ? 0xEEEE : 0x4444;
  auto prefetch = [&](int s) {
    if (w >= 4) {
#pragma unroll
      for (int y = 0; y < 4; ++y) {
        const int pc = (w - 4) * 4 + y;
        blds<true>(rws, l * 16, OFF_VOT + (s * 80 + c) * 16384 + pc * 1024, smb + ZL_VOT + (s & 1) * 16384 + pc * 1024);
      }
    }
  };
  if (w == 0) {
#pragma unroll 4
    for (int y = 0; y < 40; ++y) blds<true>(rws, l * 16, OFF_WVO + c * 327680 + y * 1024, smb + ZL_W + y * 1024);
    WAITV(0);
    const int col = l & 15, lg = l >> 4;
    const unsigned vpt = (col >> 3) * 640 + (lg * 8 + (col & 7)) * 16;
    const unsigned vpm = ((col >> 3) * 4 + lg) * 640 + 512 + (col & 7) * 16;
    unsigned budget = SPIN_MAX, budget2 = SPIN_MAX;
    for (int s = 0; s < NSTEP; ++s) {
      f4 d0, d1;
      gather_project(sm, ZL_MEM, rws, s, l, w, idx, [&](int t, int kb) {
        lptr b = sm + ZL_W + (t * 10 + kb) * 2048 + l * 16;
        return __builtin_shufflevector(lrd(b), lrd(b + 1024), 0, 1, 2, 3, 4, 5, 6, 7, 8, 9, 10, 11, 12, 13, 14, 15);
      }, d0, d1, budget);
      z_common(sm, rws, c, s, l, w, d0, d1);
      if (s + 1 == NSTEP) break;
      bf8 av[20];
#pragma unroll
      for (int t = 0; t < 16; ++t) av[t] = lrd(sm + ZL_VOT + (s & 1) * 16384 + t * 1024 + l * 16);
#pragma unroll
      for (int t = 0; t < 4; ++t) av[16 + t] = lrd(sm + ZL_VOM + t * 1024 + l * 16);
      f4 a0, a1;
      for (;;) {
        u4 pb[20];
        asm volatile("" ::: "memory");
#pragma unroll
        for (int t = 0; t < 16; ++t) pb[t] = gld128(rws, vpt, OFF_P + s * P_STEP + t * 1280);
#pragma unroll
        for (int t = 0; t < 4; ++t) pb[16 + t] = gld128(rws, vpm, OFF_P + s * P_STEP + t * 8 * 640);
        a0 = f4{0, 0, 0, 0}; a1 = f4{0, 0, 0, 0};
        unsigned mx = 0;
#pragma unroll
        for (int t = 0; t < 20; ++t) {
          mx = umax3(umax3(mx, pb[t][0], pb[t][1]), pb[t][2], pb[t][3]);
          if (t & 1) a1 = mfma16(av[t], asbf8(pb[t]), a1); else a0 = mfma16(av[t], asbf8(pb[t]), a0);
        }
        if (!__any(mx == SENT) || budget2 == 0) break;
        --budget2;
      }
      float o[4];
#pragma unroll
      for (int r = 0; r < 4; ++r) {
        float x = a0[r] + a1[r], y = x;
        swap32(x, y);
        const float z = __builtin_bit_cast(float, __builtin_amdgcn_update_dpp(0, __builtin_bit_cast(int, y), 0x128, 0xF, 0xF, true));
        o[r] = a0[r] + a1[r] + z;
      }
      if (col < 8 && lg < 2) gst64(rws, u2{nos(pk2(o[0], o[1])), nos(pk2(o[2], o[3]))}, col * 16 + lg * 8, OFF_MEM + (s + 1) * MEM_STEP + c * 128);
    }
  } else {
    bf16v wf[2][10];
#pragma unroll
    for (int t = 0; t < 2; ++t)
#pragma unroll
      for (int kb = 0; kb < 10; ++kb) {
        const unsigned so = OFF_WVO + c * 327680 + ((2 * w + t) * 10 + kb) * 2048;
        wf[t][kb] = __builtin_shufflevector(asbf8(gld128(rws, l * 16, so)), asbf8(gld128(rws, l * 16, so + 1024)), 0, 1, 2, 3, 4, 5, 6, 7, 8, 9, 10, 11, 12, 13, 14, 15);
      }
    prefetch(0);
    unsigned budget = SPIN_MAX;
    for (int s = 0; s < NSTEP; ++s) {
      f4 d0, d1;
      gather_project(sm, ZL_MEM, rws, s, l, w, idx, [&](int t, int kb) { return wf[t][kb]; }, d0, d1, budget);
      z_common(sm, rws, c, s, l, w, d0, d1);
      if (s + 1 < NSTEP) prefetch(s + 1);
      if (w <= 4) {
        const u4 v = *(const LAS u4*)(sm + ZL_VOM + (w - 1) * 1024 + l * 16);
        gst128(rws, v, l * 16, OFF_VOM + (s * 80 + c) * 4096 + (w - 1) * 1024);
      }
    }
  }
  WAITV(0);
}

extern __shared__ __attribute__((aligned(16))) char smem_[];
__global__ __launch_bounds__(512, 2) void k_prep(Params P) {
  const int i = blockIdx.x * 512 + threadIdx.x;
  if (i < 640) {
    const int c = i >> 3, r = i & 7;
    u4 v = *(const u4*)(P.Bmem0 + r * 1280 + c * 16);
    *(u4*)(P.ws + OFF_MEM + c * 128 + r * 16) = u4{nos(v[0]), nos(v[1]), nos(v[2]), nos(v[3])};
  }
}
__global__ __launch_bounds__(512, 2) void k_fold(Params P) { task_fold(P, (lptr)smem_, blockIdx.x / 5, blockIdx.x % 5); }
__global__ __launch_bounds__(512, 2) void k_bproj(Params P) { task_bproj(P, (lptr)smem_, blockIdx.x >> 5, blockIdx.x & 31); }
__global__ __launch_bounds__(512, 2) void k_a1(Params P) {
  task_gemm_rows<3, false>((lptr)smem_, mk_rsrc(P.Ax, 2048 * 1280), blockIdx.x / 5, P.Awqkv, 640 * 3840, (blockIdx.x % 5) * 384, 3840, mk_rsrc(P.ws, WS_USED), OFF_QKVA, 3840);
}
__global__ __launch_bounds__(512, 2) void k_a2(Params P) {
  const int qb = blockIdx.x >> 3, hg = blockIdx.x & 7;
  if (qb < 2) task_attn<true>(P, (lptr)smem_, qb, hg); else task_attn<false>(P, (lptr)smem_, qb, hg);
}
__global__ __launch_bounds__(512, 2) void k_a3(Params P) {
  const rsrc_t rx = mk_rsrc(P.ws + OFF_OA, 2048 * 1280), ro = mk_rsrc(P.out, 2048 * 2560);
  if (blockIdx.x & 1) task_gemm_rows<2, true>((lptr)smem_, rx, blockIdx.x >> 1, P.Awo, 640 * 1280, 384, 1280, ro, 0, 2560);
  else task_gemm_rows<3, true>((lptr)smem_, rx, blockIdx.x >> 1, P.Awo, 640 * 1280, 0, 1280, ro, 0, 2560);
}
__global__ __launch_bounds__(512, 2) void k_rec(Params P) {
  if (blockIdx.x < 32) role_x(P, (lptr)smem_, blockIdx.x); else role_z(P, (lptr)smem_, blockIdx.x - 32);
}
__global__ __launch_bounds__(512, 2) void k_tout(Params P) { task_tout(P, (lptr)smem_, blockIdx.x / 5, blockIdx.x % 5); }

constexpr int LDS_BYTES = 131072;
extern "C" void kernel_launch(void* const* d_in, const int* in_sizes, int n_in, void* d_out, int out_size, void* d_ws, size_t ws_size, hipStream_t stream) {
  Params P;
  P.Ax = (const char*)d_in[0]; P.Awqkv = (const char*)d_in[1]; P.Awo = (const char*)d_in[2]; P.Bx = (const char*)d_in[3]; P.Bmem0 = (const char*)d_in[4];
  P.Wq = (const char*)d_in[5]; P.Wk = (const char*)d_in[6]; P.Wv = (const char*)d_in[7]; P.Wo = (const char*)d_in[8];
  P.out = (char*)d_out; P.ws = (char*)d_ws;
  const void* ks[] = {(const void*)k_fold, (const void*)k_bproj, (const void*)k_a1, (const void*)k_a2, (const void*)k_a3, (const void*)k_rec, (const void*)k_tout};
  for (const void* k : ks) (void)hipFuncSetAttribute(k, hipFuncAttributeMaxDynamicSharedMemorySize, LDS_BYTES);
  (void)hipMemsetAsync(d_ws, 0xFF, INIT_BYTES, stream);
  k_prep<<<2, 512, 0, stream>>>(P);
  k_fold<<<160, 512, LDS_BYTES, stream>>>(P);
  k_bproj<<<512, 512, LDS_BYTES, stream>>>(P);
  k_a1<<<80, 512, LDS_BYTES, stream>>>(P);
  k_a2<<<128, 512, LDS_BYTES, stream>>>(P);
  k_a3<<<32, 512, LDS_BYTES, stream>>>(P);
  k_rec<<<112, 512, LDS_BYTES, stream>>>(P);
  k_tout<<<320, 512, LDS_BYTES, stream>>>(P);
}
```

```cpp
#include <hip/hip_runtime.h>
#include <hip/hip_bf16.h>
#include <stdint.h>

#define LAS __attribute__((address_space(3)))
typedef __attribute__((ext_vector_type(8))) __bf16 h8;
typedef __attribute__((ext_vector_type(4))) __bf16 h4;
typedef __attribute__((ext_vector_type(2))) __bf16 h2;
typedef __attribute__((ext_vector_type(16))) __bf16 h16;
typedef __attribute__((ext_vector_type(4))) float f4;
typedef __attribute__((ext_vector_type(4))) unsigned u4;
typedef __attribute__((ext_vector_type(2))) unsigned u2;
typedef __attribute__((ext_vector_type(4))) short s4;
typedef LAS char* lptr;
typedef __amdgpu_buffer_rsrc_t rsrc_t;

constexpr unsigned HAND_BYTES = (8u << 20) + 65536;
constexpr unsigned CTR_REL = 8u << 20;
constexpr unsigned OFF_WVO = 12u << 20;
constexpr unsigned OFF_QT = OFF_WVO + 80u * 327680u;
constexpr unsigned OFF_KT = OFF_QT + 64u * 32u * 8192u;
constexpr unsigned OFF_VOT = OFF_KT + 64u * 32u * 8192u;
constexpr unsigned OFF_VOM = OFF_VOT + 64u * 80u * 16384u;
constexpr unsigned OFF_PT = OFF_VOM + 64u * 80u * 4096u;
constexpr unsigned OFF_QKVA = OFF_PT + 64u * 32u * 2560u;
constexpr unsigned OFF_OA = OFF_QKVA + 2048u * 3840u;
constexpr unsigned WS_USED = OFF_OA + 2048u * 1280u;
static_assert(WS_USED <= (256u << 20), "workspace");
constexpr unsigned SENT = 0xFFFFFFFFu;
constexpr unsigned CTR_PAR = CTR_REL, CTR_READY = CTR_REL + 256, CTR_START = CTR_REL + 512;
constexpr unsigned CTR_ACK = CTR_REL + 1024, CTR_HELPER = CTR_REL + 1280;
constexpr unsigned CTR_PROG = CTR_REL + 2048, CTR_TOUT = CTR_REL + 2304, CTR_AQ = CTR_REL + 2560;
constexpr unsigned FL_FOLD = CTR_REL + 4096;
constexpr unsigned PR_QK = CTR_REL + 8192;
constexpr unsigned PR_VO = CTR_REL + 16384;
constexpr unsigned FL_A1 = CTR_REL + 12288;
constexpr unsigned FL_A2 = CTR_REL + 13312;
constexpr unsigned CTR_BYTES = 32768;
constexpr unsigned HS_C = CTR_REL + 32768, HS_E = CTR_REL + 36864;

struct Params {
  const char* Ax; const char* Awqkv; const char* Awo; const char* Bx; const char* Bmem0;
  const char* Wq; const char* Wk; const char* Wv; const char* Wo; char* out; char* ws;
};

__device__ __forceinline__ f4 mfma16(h8 a, h8 b, f4 c) { return __builtin_amdgcn_mfma_f32_16x16x32_bf16(a, b, c, 0, 0, 0); }
__device__ __forceinline__ f4 mfma16k16(h4 a, h4 b, f4 c) {
  return __builtin_amdgcn_mfma_f32_16x16x16bf16_1k(__builtin_bit_cast(s4, a), __builtin_bit_cast(s4, b), c, 0, 0, 0);
}
__device__ __forceinline__ f4 smfma(h8 a, h16 b, f4 c, int idx) { return __builtin_amdgcn_smfmac_f32_16x16x64_bf16(a, b, c, idx, 0, 0); }
__device__ __forceinline__ unsigned pk2(float a, float b) { h2 v = {(__bf16)a, (__bf16)b}; return __builtin_bit_cast(unsigned, v); }
__device__ __forceinline__ u4 pk8(f4 a, f4 b) { return u4{pk2(a[0], a[1]), pk2(a[2], a[3]), pk2(b[0], b[1]), pk2(b[2], b[3])}; }
__device__ __forceinline__ h8 lrd(lptr p) { return *(const LAS h8*)p; }
__device__ __forceinline__ h4 lrd64(lptr p) { return *(const LAS h4*)p; }
__device__ __forceinline__ h4 ltr(lptr p) { return __builtin_bit_cast(h4, __builtin_amdgcn_ds_read_tr16_b64_v4i16((LAS s4*)p)); }
__device__ __forceinline__ h8 cat8(h4 a, h4 b) { return __builtin_shufflevector(a, b, 0, 1, 2, 3, 4, 5, 6, 7); }
__device__ __forceinline__ h8 as_h8(u4 v) { return __builtin_bit_cast(h8, v); }
__device__ __forceinline__ rsrc_t mk_rsrc(const void* p, unsigned bytes) { return __builtin_amdgcn_make_buffer_rsrc((void*)p, 0, bytes, 0x00020000); }
template <bool SC1>
__device__ __forceinline__ void blds(rsrc_t r, unsigned voff, unsigned soff, unsigned lds) {
  if (SC1) asm volatile("s_mov_b32 m0, %0\n\ts_nop 0\n\tbuffer_load_dwordx4 %1, %2, %3 offen sc1 lds" ::"s"(lds), "v"(voff), "s"(r), "s"(soff) : "memory");
  else asm volatile("s_mov_b32 m0, %0\n\ts_nop 0\n\tbuffer_load_dwordx4 %1, %2, %3 offen lds" ::"s"(lds), "v"(voff), "s"(r), "s"(soff) : "memory");
}
__device__ __forceinline__ u4 gld128(rsrc_t r, unsigned voff, unsigned soff) { return __builtin_amdgcn_raw_buffer_load_b128(r, voff, soff, 16); }
__device__ __forceinline__ void gst128(rsrc_t r, u4 v, unsigned voff, unsigned soff) { __builtin_amdgcn_raw_buffer_store_b128(v, r, voff, soff, 16); }
__device__ __forceinline__ void gst64(rsrc_t r, u2 v, unsigned voff, unsigned soff) { __builtin_amdgcn_raw_buffer_store_b64(v, r, voff, soff, 16); }
#define WAITV(n) do { asm volatile("" ::: "memory"); __builtin_amdgcn_s_waitcnt(((n) & 15) | (((n) >> 4) << 14) | 0x0F70); asm volatile("" ::: "memory"); } while (0)
#define WAITL(n) do { asm volatile("" ::: "memory"); __builtin_amdgcn_s_waitcnt(((n) << 8) | 0xC07F); asm volatile("" ::: "memory"); } while (0)
#define BAR() do { asm volatile("" ::: "memory"); __builtin_amdgcn_s_barrier(); asm volatile("" ::: "memory"); } while (0)
#define SCHEDB() __builtin_amdgcn_sched_barrier(0)
__device__ __forceinline__ unsigned umin32(unsigned a, unsigned b) { return a < b ? a : b; }
__device__ __forceinline__ unsigned umax3(unsigned a, unsigned b, unsigned c) { unsigned m = a > b ? a : b; return m > c ? m : c; }
__device__ __forceinline__ unsigned nos(unsigned x) { unsigned r; asm("v_pk_min_u16 %0, %1, -2 op_sel_hi:[1,0]" : "=v"(r) : "v"(x)); return r; }
__device__ __forceinline__ bool unwritten(unsigned x) { return (x & 0xFFFFu) == 0xFFFFu || x >= 0xFFFF0000u; }
__device__ __forceinline__ unsigned pkmax(unsigned a, unsigned b) { unsigned r; asm("v_pk_max_u16 %0, %1, %2" : "=v"(r) : "v"(a), "v"(b)); return r; }
__device__ __forceinline__ unsigned pkmax4(unsigned m, u4 v) { return pkmax(pkmax(m, pkmax(v[0], v[1])), pkmax(v[2], v[3])); }
__device__ __forceinline__ void swap32(float& a, float& b) {
  auto r = __builtin_amdgcn_permlane32_swap(__builtin_bit_cast(unsigned, a), __builtin_bit_cast(unsigned, b), false, false);
  const unsigned r0 = r[0], r1 = r[1];
  a = __builtin_bit_cast(float, r0); b = __builtin_bit_cast(float, r1);
}
__device__ __forceinline__ void swap16u(unsigned& a, unsigned& b) {
  auto r = __builtin_amdgcn_permlane16_swap(a, b, false, false);
  a = r[0]; b = r[1];
}
__device__ __forceinline__ float xmax4(float x) {
  unsigned a = __builtin_bit_cast(unsigned, x), b = a;
  swap16u(a, b);
  float y = __builtin_elementwise_maximum(__builtin_bit_cast(float, a), __builtin_bit_cast(float, b)), z = y;
  swap32(y, z);
  return __builtin_elementwise_maximum(y, z);
}
__device__ __forceinline__ float xsum4(float x) {
  unsigned a = __builtin_bit_cast(unsigned, x), b = a;
  swap16u(a, b);
  float y = __builtin_bit_cast(float, a) + __builtin_bit_cast(float, b), z = y;
  swap32(y, z);
  return y + z;
}
constexpr float NEG_INF = -__builtin_huge_valf();
__device__ __forceinline__ int TID() { int t = threadIdx.x; asm volatile("" : "+v"(t)); return t; }

constexpr int STG = 32768;
constexpr int LDS_V = 3 * STG;

template <int NB, bool XSC1, class After>
__device__ __forceinline__ void gemm_p1(lptr sm, rsrc_t rx, unsigned xrow0, rsrc_t r0, rsrc_t r1, rsrc_t r2, unsigned wcol_bytes, unsigned ldw2,
                                        f4 (&acc)[4][2 * NB], After after) {
  const int tid = TID(), l = tid & 63, w = __builtin_amdgcn_readfirstlane(tid >> 6);
  const int tg = w & 1, cg = w >> 1;
  const unsigned smb = (unsigned)(size_t)sm;
  const int rho = l >> 2;
  const unsigned vx = (xrow0 + 32 * (w >> 1) + 8 * (rho >> 2) + 4 * (w & 1) + (rho & 3)) * 1280u + (((l & 3) ^ (((rho >> 3) & 1) << 1)) * 16);
  const int grow = 8 * (w & 3) + ((l >> 2) & 7);
  const unsigned vw = grow * ldw2 + (64 * (w >> 2) + 32 * (l >> 5) + 8 * ((l & 3) ^ ((grow >> 2) & 3))) * 2 + wcol_bytes;
  auto stage = [&](int s) {
    const unsigned b = smb + (s % 3) * STG;
    blds<XSC1>(rx, vx, s * 64, b + w * 1024);
    const unsigned ko = (unsigned)s * 32u * ldw2;
    const unsigned bw = b + 8192 + (w >> 2) * 4096 + (w & 3) * 1024;
    blds<false>(r0, vw, ko, bw);
    blds<false>(r1, vw, ko, bw + 8192);
    if (NB == 3) blds<false>(r2, vw, ko, bw + 16384);
  };
  const int g = l >> 4, q = (l & 15) >> 2, p = l & 3;
  const int ax = (4 * tg) * 1024 + (l & 15) * 64 + ((g ^ ((((l & 15) >> 3) & 1) << 1)) * 16);
  int aw[2][2];
#pragma unroll
  for (int t = 0; t < 2; ++t)
#pragma unroll
    for (int b = 0; b < 2; ++b)
      aw[t][b] = 8192 + 4096 * (cg >> 1) + 512 * (cg & 1) + 1024 * g + 64 * (4 * t + q) + 16 * ((2 * b + (p >> 1)) ^ ((2 * g + t) & 3)) + 8 * (p & 1);
  struct Fr { h8 x[4]; h8 w[2 * NB]; };
  auto rd = [&](Fr& f, int s) {
    lptr b = sm + (s % 3) * STG;
#pragma unroll
    for (int i = 0; i < 4; ++i) f.x[i] = lrd(b + ax + i * 1024);
#pragma unroll
    for (int j = 0; j < 2 * NB; ++j) f.w[j] = cat8(ltr(b + aw[0][j & 1] + 8192 * (j >> 1)), ltr(b + aw[1][j & 1] + 8192 * (j >> 1)));
  };
#pragma unroll
  for (int i = 0; i < 4; ++i)
#pragma unroll
    for (int j = 0; j < 2 * NB; ++j) acc[i][j] = f4{0, 0, 0, 0};
  stage(0); stage(1); stage(2);
  if (NB == 3) WAITV(8); else WAITV(6);
  BAR();
  Fr f[2];
  rd(f[0], 0);
#pragma unroll
  for (int t = 0; t < 20; ++t) {
    if (t + 1 < 20) {
      if (NB == 3) { if (t + 2 < 20 || After::N > 0) WAITV(4); else WAITV(0); }
      else { if (t + 2 < 20) WAITV(3); else WAITV(0); }
      WAITL(0); BAR();
      if (t + 3 < 20) stage(t + 3); else after.stage(t + 3);
      rd(f[(t + 1) & 1], t + 1);
    }
#pragma unroll
    for (int j = 0; j < 2 * NB; ++j)
#pragma unroll
      for (int i = 0; i < 4; ++i) acc[i][j] = mfma16(f[t & 1].w[j], f[t & 1].x[i], acc[i][j]);
    if (t + 1 < 20) {
#pragma unroll
      for (int k = 0; k < 4 + 4 * NB; ++k) { __builtin_amdgcn_sched_group_barrier(0x008, 1, 0); __builtin_amdgcn_sched_group_barrier(0x100, 1, 0); }
    }
    SCHEDB();
  }
}
struct NoAfter { static constexpr int N = 0; __device__ __forceinline__ void stage(int) const {} };

constexpr int WS_XB = 40960, WS_RING = 2 * WS_XB, WS_SLOT = 16384, WS_V = 3 * WS_XB, WS_DUM = WS_V + 16384;
constexpr int LOOK = 3;
constexpr int WARM = 5;
__device__ __forceinline__ unsigned gld32(rsrc_t r, unsigned voff, unsigned soff) { return __builtin_amdgcn_raw_buffer_load_b32(r, voff, soff, 16); }
__device__ __forceinline__ void gst32(rsrc_t r, unsigned v, unsigned voff, unsigned soff) { __builtin_amdgcn_raw_buffer_store_b32(v, r, voff, soff, 16); }
constexpr unsigned OOB = 0x80000000u;
constexpr int POLL_MAX = 1 << 21;
constexpr int LDS_BYTES = 163840, LDS_SCHED = LDS_BYTES - 256;

struct Hs {
  char* ws; unsigned hb, clo, chi, rot; bool ok;
  __device__ __forceinline__ void echo(int l) const {
    const rsrc_t rws = mk_rsrc(ws, WS_USED);
    const u4 a = gld128(rws, l * 16, hb + HS_C), b = gld128(rws, l * 16, hb + HS_C + 1024);
    gst128(rws, u4{(a[0] & ~7u) | rot, a[1], (a[2] & ~7u) | rot, a[3]}, l * 16, hb + HS_E);
    gst128(rws, u4{(b[0] & ~7u) | rot, b[1], (b[2] & ~7u) | rot, b[3]}, l * 16, hb + HS_E + 1024);
  }
  __device__ __forceinline__ u2 peek() const {
    return __builtin_amdgcn_raw_buffer_load_b64(mk_rsrc(ws, WS_USED), 0, hb + (blockIdx.x == 0 ? CTR_HELPER : HS_E + blockIdx.x * 8), 16);
  }
  template <bool FIRST = true, bool POSTBAR = false>
  __device__ __forceinline__ bool check(lptr sm, u2 ev, int woff = LDS_SCHED + 16) {
    if (ok) return true;
    volatile LAS unsigned* word = (volatile LAS unsigned*)(sm + woff);
    if (FIRST) {
      const int tid = TID();
      if (__builtin_amdgcn_readfirstlane(tid >> 6) == 0) {
        unsigned lo = __builtin_amdgcn_readfirstlane(ev[0]), hi = __builtin_amdgcn_readfirstlane(ev[1]);
        for (int i = 0; i < POLL_MAX; ++i) {
          if (blockIdx.x == 0) { if (lo != SENT) { lo = rot; break; } echo(tid); }
          else { if (((lo ^ clo) >> 3) == 0 && hi == chi) break; __builtin_amdgcn_s_sleep(4); }
          const u2 v = peek();
          lo = __builtin_amdgcn_readfirstlane(v[0]); hi = __builtin_amdgcn_readfirstlane(v[1]);
        }
        if (tid == 0) {
          *word = lo & 7u;
          (void)__hip_atomic_fetch_add((unsigned*)(ws + hb + CTR_ACK), 1u, __ATOMIC_RELAXED, __HIP_MEMORY_SCOPE_AGENT);
        }
        WAITL(0);
      }
    }
    BAR();
    const unsigned r = __builtin_amdgcn_readfirstlane(*word);
    ok = true;
    if (r == rot) { if (POSTBAR) BAR(); return true; }
    rot = r;
    WAITV(0); WAITL(0); BAR();
    return false;
  }
};
struct WsLane {
  int l, w; unsigned smb, vx, pcol, plds; int lam, ax, aw[2][2];
  __device__ __forceinline__ void init(lptr sm, int tid, int cg, int b_fixed) {
    l = tid & 63; w = __builtin_amdgcn_readfirstlane(tid >> 6);
    smb = (unsigned)(size_t)sm;
    const int rho = l >> 2;
    vx = (8 * (rho >> 2) + 4 * (w & 1) + (rho & 3)) * 1280u + (((l & 3) ^ (((rho >> 3) & 1) << 1)) * 16);
    lam = 8 * (w & 3) + ((l >> 2) & 7);
    pcol = (64 * (w >> 2) + 32 * (l >> 5) + 8 * ((l & 3) ^ ((lam >> 2) & 3))) * 2;
    plds = smb + WS_RING + (w >> 2) * 4096 + (w & 3) * 1024;
    const int g = l >> 4, q = (l & 15) >> 2, p = l & 3;
    ax = (l & 15) * 64 + ((g ^ ((((l & 15) >> 3) & 1) << 1)) * 16);
#pragma unroll
    for (int t = 0; t < 2; ++t)
#pragma unroll
      for (int b = 0; b < 2; ++b) {
        const int bb = b_fixed >= 0 ? b_fixed : b, c2 = b_fixed >= 0 ? cg + 2 * b : cg;
        aw[t][b] = WS_RING + 4096 * (c2 >> 1) + 512 * (c2 & 1) + 1024 * g + 64 * (4 * t + q) + 16 * ((2 * bb + (p >> 1)) ^ ((2 * g + t) & 3)) + 8 * (p & 1);
      }
  }
  __device__ __forceinline__ h8 wfrag(lptr sm, int slot, int piece, int b) const {
    lptr base = sm + (slot % 5) * WS_SLOT + piece * 8192;
    return cat8(ltr(base + aw[0][b]), ltr(base + aw[1][b]));
  }
  __device__ __forceinline__ void xload(rsrc_t rx, int seg, int n) const { blds<false>(rx, vx, seg * 40960 + (n >> 1) * 64, smb + (seg % 3) * WS_XB + n * 1024); }
};
template <int NL, int NS, int EARLY, class Epi, class Pub>
__device__ __forceinline__ void ws_stream(lptr sm, const WsLane& L, rsrc_t rx, const h8 (&wf)[2][20], Epi epi, Pub pub) {
  h8 xr[4][2];
  int cur = L.ax, nxt = L.ax + WS_XB;
  const unsigned vwarm = L.l < 40 ? (L.w * 40 + L.l) * 128 : OOB;
#pragma unroll
  for (int k = 0; k < LOOK; ++k) { xr[k][0] = lrd(sm + cur + (2 * k) * 1024); xr[k][1] = lrd(sm + cur + (2 * k + 1) * 1024); }
#pragma unroll 1
  for (int t = 0; t < 64; ++t) {
    f4 acc[2][2];
    const int sn = t + 2 < 64 ? t + 2 : 63, sw = t + WARM < 64 ? t + WARM : 63;
    const unsigned so = sn * 40960, lb = L.smb + ((t + 2) % 3) * WS_XB;
#pragma unroll
    for (int kb = 0; kb < 20; ++kb) {
      if (kb == 10) { if (t < EARLY) WAITV(0); else WAITV(NS + 1); BAR(); pub(t); }
      const int kn = kb + LOOK;
#pragma unroll
      for (int i = 0; i < 2; ++i) xr[kn & 3][i] = lrd(sm + (kn < 20 ? cur + (2 * kn + i) * 1024 : nxt + (2 * (kn - 20) + i) * 1024));
      if (kb >= 10 && (NL == 10 || ((kb & 1) != 0) == (L.w >= 4))) {
        const int n = L.w % (40 / NL) + (40 / NL) * (NL == 10 ? kb - 10 : (kb - 10) >> 1);
        blds<false>(rx, L.vx, so + (n >> 1) * 64, lb + n * 1024);
      }
      if (kb == 19) asm volatile("s_mov_b32 m0, %0\n\ts_nop 0\n\tbuffer_load_dword %1, %2, %3 offen lds" ::"s"(L.smb + WS_DUM + L.w * 256), "v"(vwarm), "s"(rx), "s"(sw * 40960) : "memory");
#pragma unroll
      for (int i = 0; i < 2; ++i)
#pragma unroll
        for (int b = 0; b < 2; ++b) acc[i][b] = mfma16(wf[b][kb], xr[kb & 3][i], kb == 0 ? f4{0, 0, 0, 0} : acc[i][b]);
      SCHEDB();
    }
    epi(t, acc);
    cur = nxt; nxt = nxt + WS_XB >= L.ax + 3 * WS_XB ? L.ax : nxt + WS_XB;
  }
}
__device__ __forceinline__ bool role_qk(const Params& P, lptr sm, int h, unsigned hb, Hs& hs) {
  WsLane L; { const int tid = TID(); L.init(sm, tid, (tid >> 6) & 3, -1); }
  const int l = L.l, w = L.w;
  const rsrc_t rx = mk_rsrc(P.Bx, 2048 * 1280), rq = mk_rsrc(P.Wq, 640 * 8192), rk = mk_rsrc(P.Wk, 640 * 8192), rws = mk_rsrc(P.ws, WS_USED);
#pragma unroll
  for (int y = 0; y < 10; ++y) L.xload(rx, y / 5, w + 8 * (y % 5));
  const unsigned vw = L.lam * 8192 + L.pcol + h * 256;
  auto stage = [&](int s) {
    blds<false>(rq, vw, s * 32 * 8192, L.plds + (s % 5) * WS_SLOT);
    blds<false>(rk, vw, s * 32 * 8192, L.plds + (s % 5) * WS_SLOT + 8192);
  };
  h8 wf[2][20];
  u2 ev = u2{0, 0};
  stage(0); stage(1); stage(2); stage(3);
#pragma unroll
  for (int s = 0; s < 20; ++s) {
    if (s + 3 < 20) WAITV(6); else if (s + 2 < 20) WAITV(4); else if (s + 1 < 20) WAITV(2); else WAITV(0);
    BAR();
    if (s + 4 < 20) stage(s + 4);
    if (s == 16 && w == 0) ev = hs.peek();
    wf[0][s] = L.wfrag(sm, s, w >> 2, 0); wf[1][s] = L.wfrag(sm, s, w >> 2, 1);
    WAITL(0);
  }
  BAR();
  if (!hs.check(sm, ev)) return false;
  const unsigned obase = (w < 4 ? OFF_QT : OFF_KT) + (h * 8 + (w & 3)) * 1024, vpub = l == 0 && w == 0 ? 0 : OOB;
  ws_stream<5, 2, 3>(sm, L, rx, wf, [&](int t, f4 (&acc)[2][2]) {
#pragma unroll
    for (int i = 0; i < 2; ++i) gst128(rws, pk8(acc[i][0], acc[i][1]), l * 16, obase + t * 32 * 8192 + i * 4096);
  }, [&](int t) { if (w == 0 && t >= 1) gst32(rws, (unsigned)(t < 3 ? t - 1 : t - 2), vpub, hb + PR_QK + h * 128); });
  WAITV(0); BAR();
  if (w == 0) gst32(rws, 63u, vpub, hb + PR_QK + h * 128);
  return true;
}
__device__ __forceinline__ bool role_vo(const Params& P, lptr sm, int h, unsigned hb, Hs& hs) {
  WsLane L; { const int tid = TID(), w = tid >> 6; L.init(sm, tid, w < 4 ? w : (w - 4) >> 1, w < 4 ? -1 : (w & 1)); }
  const int l = L.l, w = L.w;
  const rsrc_t rx = mk_rsrc(P.Bx, 2048 * 1280), rv = mk_rsrc(P.Wv, 640 * 8192), rwo = mk_rsrc(P.Wo, 4096 * 1280), rws = mk_rsrc(P.ws, WS_USED);
#pragma unroll
  for (int y = 0; y < 10; ++y) L.xload(rx, y / 5, w + 8 * (y % 5));
  const unsigned vw = L.lam * 8192 + L.pcol + h * 256;
  const unsigned vwo = (h * 128 + 16 * ((L.lam >> 2) & 1) + 4 * (L.lam >> 3) + (L.lam & 3)) * 1280u + L.pcol;
  auto stage = [&](int s) {
#pragma unroll
    for (int u = 0; u < 2; ++u) {
      if (s < 10) blds<false>(rv, vw, (2 * s + u) * 32 * 8192, L.plds + (s % 5) * WS_SLOT + u * 8192);
      else blds<false>(rwo, vwo, (2 * ((s - 10) & 1) + u) * 32 * 1280 + ((s - 10) >> 1) * 256, L.plds + (s % 5) * WS_SLOT + u * 8192);
    }
  };
  auto step_head = [&](int s) {
    if (s + 3 < 20) WAITV(6); else if (s + 2 < 20) WAITV(4); else if (s + 1 < 20) WAITV(2); else WAITV(0);
    BAR();
    if (s + 4 < 20) stage(s + 4);
  };
  const unsigned vpub = l == 0 ? 0 : OOB;
  stage(0); stage(1); stage(2); stage(3);
  if (w < 4) {
    L.init(sm, TID(), w, -1);
    h8 wf[2][20];
    u2 ev = u2{0, 0};
#pragma unroll
    for (int s = 0; s < 20; ++s) {
      step_head(s);
      if (s == 16 && w == 0) ev = hs.peek();
      if (s < 10) {
#pragma unroll
        for (int u = 0; u < 2; ++u) { wf[0][2 * s + u] = L.wfrag(sm, s, u, 0); wf[1][2 * s + u] = L.wfrag(sm, s, u, 1); }
      }
      WAITL(0);
    }
    BAR();
    if (!hs.check(sm, ev)) return false;
    ws_stream<10, 0, 0>(sm, L, rx, wf, [&](int t, f4 (&acc)[2][2]) {
#pragma unroll
      for (int i = 0; i < 2; ++i) *(LAS u4*)(sm + WS_V + (t & 1) * 8192 + (i * 4 + w) * 1024 + l * 16) = pk8(acc[i][0], acc[i][1]);
    }, [&](int t) { if (w == 0 && t >= 2) gst32(rws, (unsigned)(t < 4 ? t - 2 : t - 3), vpub, hb + PR_VO + h * 4); });
    WAITL(0); BAR();
  } else {
    L.init(sm, TID(), (w - 4) >> 1, w & 1);
    h8 wo[5][2][4];
#pragma unroll
    for (int s = 0; s < 20; ++s) {
      step_head(s);
      if (s >= 10) {
#pragma unroll
        for (int u = 0; u < 2; ++u)
#pragma unroll
          for (int ts = 0; ts < 2; ++ts) wo[(s - 10) >> 1][ts][2 * ((s - 10) & 1) + u] = L.wfrag(sm, s, u, ts);
      }
      WAITL(0);
    }
    BAR();
    if (!hs.check<false>(sm, u2{0, 0})) return false;
    const int j = w - 4;
    const unsigned vo = (l >> 4) * 256 + (h & 1) * 128 + (l & 7) * 16 + ((l >> 3) & 1) * 16384;
    const unsigned ob = OFF_VOT + ((4 * (j >> 1) + 2 * (j & 1)) * 16 + (h >> 1)) * 1024;
    BAR();
#pragma unroll 1
    for (int u = 0; u < 64; ++u) {
      if (u < 3) WAITV(0); else WAITV(10);
      BAR();
      h8 a[2][4];
#pragma unroll
      for (int i = 0; i < 2; ++i)
#pragma unroll
        for (int m = 0; m < 4; ++m) a[i][m] = lrd(sm + WS_V + (u & 1) * 8192 + (i * 4 + m) * 1024 + l * 16);
#pragma unroll
      for (int pi = 0; pi < 5; ++pi) {
        f4 c[2][2];
#pragma unroll
        for (int m = 0; m < 4; ++m)
#pragma unroll
          for (int ts = 0; ts < 2; ++ts)
#pragma unroll
            for (int i = 0; i < 2; ++i) c[ts][i] = mfma16(a[i][m], wo[pi][ts][m], m == 0 ? f4{0, 0, 0, 0} : c[ts][i]);
#pragma unroll
        for (int ts = 0; ts < 2; ++ts) gst128(rws, pk8(c[ts][0], c[ts][1]), vo, ob + u * (80 * 16384) + (16 * pi + 8 * ts) * 16384);
      }
    }
  }
  WAITV(0); BAR();
  if (w == 0) gst32(rws, 63u, vpub, hb + PR_VO + h * 4);
  return true;
}

__device__ __forceinline__ int mem_line(int c) { return 20 * ((c >> 2) & 3) + 4 * (c >> 4) + (c & 3); }
__device__ __forceinline__ int mem_group(int n) { const int x = n / 20, i = n - 20 * x; return 16 * (i >> 2) + 4 * x + (i & 3); }
constexpr int FLAG = 2, FLAG1 = 1;
template <int V> struct IC { static constexpr int value = V; };
struct FoldPlan { int wait[10], ann[10], load[10], tail; };
constexpr FoldPlan fold_plan(int nu) {
  FoldPlan p = {};
  int pos = 0, endb[5] = {}, ends[10] = {};
  for (int b = 0; b < 5 - nu; ++b) { pos += 4; endb[b] = pos; }
  p.tail = 0;
  for (int k = 0; k < 5 * nu; ++k) {
    const int an = k - nu - (nu == 2 ? FLAG : FLAG1);
    p.ann[k] = an >= 0 && an % nu == 0 ? an / nu : -1;
    if (p.ann[k] >= 0) p.tail = p.ann[k] + 1;
    p.load[k] = nu == 2 ? (k == 2 ? 3 : k == 4 ? 4 : -1) : (k == 1 ? 4 : -1);
    int req = 0;
    if (k > 0 && k % nu == 0) req = endb[k / nu];
    if (p.ann[k] >= 0 && ends[nu * p.ann[k] + nu - 1] > req) req = ends[nu * p.ann[k] + nu - 1];
    p.wait[k] = req > 0 ? pos - req : -1;
    if (p.load[k] >= 0) { pos += 4; endb[p.load[k]] = pos; }
    pos += 4; ends[k] = pos;
  }
  return p;
}
template <int NU>
__device__ __forceinline__ bool task_fold(const Params& P, lptr sm, int fi, int h, int db, unsigned hb, Hs& hs, bool helper) {
  const int tid = TID(), l = tid & 63, w = __builtin_amdgcn_readfirstlane(tid >> 6);
  const int tg = w & 1, cg = w >> 1;
  const unsigned smb = (unsigned)(size_t)sm;
  const rsrc_t rv = mk_rsrc(P.Wv, 640 * 8192), rwo = mk_rsrc(P.Wo, 4096 * 1280), rws = mk_rsrc(P.ws, WS_USED);
  const int lam = 8 * (w & 3) + ((l >> 2) & 7);
  const unsigned vwo = (h * 128 + lam) * 1280u + (64 * (w >> 2) + 32 * (l >> 5) + 8 * ((l & 3) ^ ((lam >> 2) & 3))) * 2;
  auto block = [&](int b) {
#pragma unroll
    for (int u = 0; u < 4; ++u) blds<false>(rwo, vwo, b * 256 + u * 32 * 1280, smb + b * STG + (w >> 2) * 4096 + (w & 3) * 1024 + u * 8192);
  };
  auto swz = [](int row) { return (row & 1) | ((row >> 1) & 14); };
#pragma unroll
  for (int un = 0; un < NU; ++un)
#pragma unroll
    for (int y = 0; y < 4; ++y) {
      const int pc = w + 8 * y, row = 4 * pc + (l >> 4);
      blds<false>(rv, (row & 7) * 8192 + h * 256 + (((l & 15) ^ swz(row)) * 16), mem_group(16 * (db + un) + (pc >> 1)) * (8 * 8192), smb + (5 - NU + un) * STG + pc * 1024);
    }
#pragma unroll
  for (int b = 0; b < 5 - NU; ++b) block(b);
  WAITV(4 * (4 - NU)); BAR();
  h8 a[NU][4][4];
  {
    const int rho = l & 15, lg = rho >> 2, r = rho & 3;
#pragma unroll
    for (int un = 0; un < NU; ++un)
#pragma unroll
      for (int t = 0; t < 4; ++t) {
        const int row = 64 * tg + 16 * (2 * (t >> 1) + (lg >> 1)) + 8 * ((r >> 1) & 1) + 4 * (lg & 1) + 2 * (t & 1) + (r & 1);
#pragma unroll
        for (int m = 0; m < 4; ++m) a[un][t][m] = lrd(sm + (5 - NU + un) * STG + row * 256 + (((4 * m + (l >> 4)) ^ swz(row)) * 16));
      }
  }
  WAITL(0); BAR();
  u2 ev = u2{0, 0};
  const unsigned vo = (h & 1) * 1024 + (l >> 4) * 128 + (l & 7) * 16 + ((l >> 3) & 1) * 327680, vpub = l == 0 ? 0 : OOB;
  constexpr int PARTB = 512;
  const int g = l >> 4, q = (l & 15) >> 2, p = l & 3;
  int aw[2][2];
#pragma unroll
  for (int t = 0; t < 2; ++t)
#pragma unroll
    for (int b = 0; b < 2; ++b)
      aw[t][b] = 4096 * (cg >> 1) + 512 * (cg & 1) + 1024 * g + 64 * (4 * t + q) + 16 * ((2 * b + (p >> 1)) ^ ((2 * g + t) & 3)) + 8 * (p & 1);
  auto announce = [&](int pi) { if (w == 0) gst32(rws, 1u, vpub, hb + FL_FOLD + (pi * 128 + fi) * 4); };
  h8 wf[4][2];
  bool good = true;
  auto step = [&](auto kc) {
    constexpr int k = decltype(kc)::value, un = k % NU, pi = k / NU;
    constexpr FoldPlan plan = fold_plan(NU);
    if constexpr (k < 5 * NU) {
      if (!good) return;
      if constexpr (plan.wait[k] >= 0) { WAITV(plan.wait[k]); BAR(); }
      if (k == 0 && w == 0) ev = hs.peek();
      if constexpr (plan.ann[k] == 0) {
        if (!hs.check<true, true>(sm, ev, 0)) { good = false; return; }
        if (helper && w == 0) gst32(rws, 1u, vpub, hb + CTR_HELPER);
      }
      if constexpr (plan.ann[k] >= 0) announce(plan.ann[k]);
      if constexpr (plan.load[k] >= 0) block(plan.load[k]);
      lptr b = sm + pi * STG;
      if (un == 0) {
#pragma unroll
        for (int m = 0; m < 4; ++m)
#pragma unroll
          for (int bb = 0; bb < 2; ++bb) wf[m][bb] = cat8(ltr(b + m * 8192 + aw[0][bb]), ltr(b + m * 8192 + aw[1][bb]));
      }
      f4 acc[4][2];
#pragma unroll
      for (int m = 0; m < 4; ++m)
#pragma unroll
        for (int bb = 0; bb < 2; ++bb)
#pragma unroll
          for (int i = 0; i < 4; ++i) acc[i][bb] = mfma16(a[un][i][m], wf[m][bb], m == 0 ? f4{0, 0, 0, 0} : acc[i][bb]);
      const int kb = 2 * (db + un) + tg;
#pragma unroll
      for (int bb = 0; bb < 2; ++bb) {
        const int c0 = 16 * pi + 4 * cg + 2 * bb;
#pragma unroll
        for (int part = 0; part < 2; ++part)
          gst128(rws, pk8(acc[2 * part][bb], acc[2 * part + 1][bb]), vo, OFF_WVO + c0 * 327680 + ((h >> 1) * 10 + kb) * 2048 + part * PARTB);
      }
    }
  };
  step(IC<0>{}); step(IC<1>{}); step(IC<2>{}); step(IC<3>{}); step(IC<4>{}); step(IC<5>{}); step(IC<6>{}); step(IC<7>{}); step(IC<8>{}); step(IC<9>{});
  if (!good) return false;
  auto tail = [&](auto pc) {
    constexpr int pi = decltype(pc)::value;
    if constexpr (pi >= fold_plan(NU).tail) { WAITV(4 * NU * (4 - pi)); BAR(); announce(pi); }
  };
  tail(IC<1>{}); tail(IC<2>{}); tail(IC<3>{}); tail(IC<4>{});
  return true;
}

__device__ __forceinline__ u4 rowpack(f4 t0, f4 t1) {
  unsigned a0 = pk2(t0[0], t0[1]), a1 = pk2(t0[2], t0[3]), b0 = pk2(t1[0], t1[1]), b1 = pk2(t1[2], t1[3]);
  swap16u(a0, b0); swap16u(a1, b1);
  return u4{a0, a1, b0, b1};
}
__device__ __forceinline__ int rowpack_col(int lg) { return 16 * (lg & 1) + 8 * (lg >> 1); }

template <int NB, bool XSC1>
__device__ __forceinline__ void task_gemm_rows(lptr sm, rsrc_t rx, int blk, const char* wbase, unsigned wbytes, unsigned col0, unsigned ldw2, rsrc_t ro, unsigned obase, unsigned ldo2) {
  const int tid = TID(), l = tid & 63, w = __builtin_amdgcn_readfirstlane(tid >> 6);
  const int tg = w & 1, cg = w >> 1;
  f4 acc[4][2 * NB];
  gemm_p1<NB, XSC1>(sm, rx, 128 * blk, mk_rsrc(wbase, wbytes), mk_rsrc(wbase + 256, wbytes - 256), mk_rsrc(wbase + 512, wbytes - 512), col0 * 2, ldw2, acc, NoAfter{});
#pragma unroll
  for (int jj = 0; jj < NB; ++jj) {
    const int pr = 4 * jj + cg;
#pragma unroll
    for (int i = 0; i < 4; ++i) {
      const int tt = 4 * tg + i, rho = l & 15;
      const int tok = 128 * blk + 32 * (tt >> 1) + 8 * (rho >> 2) + 4 * (tt & 1) + (rho & 3);
      gst128(ro, rowpack(acc[i][2 * jj], acc[i][2 * jj + 1]), tok * ldo2 + rowpack_col(l >> 4) * 2, obase + (col0 + 32 * pr) * 2);
    }
  }
}

constexpr int A2_K = 0, A2_V = 61440, A2_ONE = 122880;
template <bool EDGE>
__device__ __forceinline__ void task_attn(const Params& P, lptr sm, int qb, int hg) {
  const int tid = TID(), l = tid & 63, w = __builtin_amdgcn_readfirstlane(tid >> 6);
  const unsigned smb = (unsigned)(size_t)sm;
  const rsrc_t rws = mk_rsrc(P.ws, WS_USED);
  const int q0 = 128 * qb, key0 = q0 - 256;
#pragma unroll
  for (int y = 0; y < 15; ++y) {
    const int pc = w + 8 * y;
    const int u = (pc % 60) * 64 + l, row = u / 10, un = u - row * 10;
    int tok = key0 + row; if (EDGE) tok = tok < 0 ? 0 : tok;
    blds<true>(rws, tok * 3840 + un * 16 + (pc < 60 ? 1280 : 2560) + hg * 160, OFF_QKVA, smb + pc * 1024);
  }
  { unsigned one = 0x3F80u, zero = 0u; asm volatile("" : "+v"(one), "+v"(zero)); if (tid == 0) *(LAS u2*)(sm + A2_ONE) = u2{one, zero}; }
  const int col = l & 15, lg = l >> 4;
  const int qw = q0 + 16 * w;
  h8 qf[4];
#pragma unroll
  for (int hh = 0; hh < 4; ++hh) {
    const unsigned base = (qw + col) * 3840 + (4 * hg + hh) * 40;
    const int c0 = lg < 2 ? 2 * lg : 4, c1 = lg < 2 ? 2 * lg + 1 : 4;
    u2 a = __builtin_amdgcn_raw_buffer_load_b64(rws, base + c0 * 8, OFF_QKVA, 16);
    u2 b = __builtin_amdgcn_raw_buffer_load_b64(rws, base + c1 * 8, OFF_QKVA, 16);
    if (lg == 3) a = u2{0, 0};
    if (lg >= 2) b = u2{0, 0};
    qf[hh] = as_h8(u4{a[0], a[1], b[0], b[1]});
  }
  WAITV(0); WAITL(0); BAR();
  const float cs = 0.22360679774997896f * 1.4426950408889634f;
  const int q = (l & 15) >> 2, p = l & 3;
#pragma unroll 1
  for (int hh = 0; hh < 4; ++hh) {
    h8 qv = hh == 0 ? qf[0] : hh == 1 ? qf[1] : hh == 2 ? qf[2] : qf[3];
    lptr kb = sm + A2_K + (16 * w + col) * 160 + hh * 40;
    const int kc0 = (lg < 2 ? 2 * lg : lg == 2 ? 4 : 0) * 8, kc1 = (lg < 2 ? 2 * lg + 1 : lg == 2 ? 3 : 1) * 8;
    f4 s[17];
#pragma unroll
    for (int kt = 0; kt < 17; ++kt) s[kt] = mfma16(cat8(lrd64(kb + kt * 2560 + kc0), lrd64(kb + kt * 2560 + kc1)), qv, f4{0, 0, 0, 0});
#pragma unroll
    for (int r = 0; r < 4; ++r) {
      if (!(4 * lg + r > col)) s[0][r] = NEG_INF;
      if (!(col >= 4 * lg + r)) s[16][r] = NEG_INF;
    }
    if (EDGE) {
      const int kt0 = 16 - (qw >> 4);
#pragma unroll
      for (int kt = 0; kt < 16; ++kt) if (kt < kt0) s[kt] = f4{NEG_INF, NEG_INF, NEG_INF, NEG_INF};
    }
    float m = s[16][0];
#pragma unroll
    for (int kt = 0; kt < 17; ++kt) m = fmaxf(fmaxf(m, fmaxf(s[kt][0], s[kt][1])), fmaxf(s[kt][2], s[kt][3]));
    m = xmax4(m);
    const float mc = m * cs;
    h4 pb[17];
#pragma unroll
    for (int kt = 0; kt < 17; ++kt) {
      float e[4];
#pragma unroll
      for (int r = 0; r < 4; ++r) e[r] = __builtin_amdgcn_exp2f(__builtin_fmaf(s[kt][r], cs, -mc));
      pb[kt] = __builtin_bit_cast(h4, u2{pk2(e[0], e[1]), pk2(e[2], e[3])});
    }
    lptr vb0 = sm + A2_V + (16 * w + 4 * lg + q) * 160 + hh * 40 + p * 8;
    lptr vb1 = p == 0 ? sm + A2_V + (16 * w + 4 * lg + q) * 160 + hh * 40 + 32 : sm + A2_ONE;
    const int st1 = p == 0 ? 2560 : 0;
    f4 o0 = {0, 0, 0, 0}, o1 = {0, 0, 0, 0};
#pragma unroll
    for (int m2 = 0; m2 < 8; ++m2) {
      const h8 pp = cat8(pb[2 * m2], pb[2 * m2 + 1]);
      o0 = mfma16(cat8(ltr(vb0 + (2 * m2) * 2560), ltr(vb0 + (2 * m2 + 1) * 2560)), pp, o0);
      o1 = mfma16(cat8(ltr(vb1 + (2 * m2) * st1), ltr(vb1 + (2 * m2 + 1) * st1)), pp, o1);
    }
    o0 = mfma16k16(ltr(vb0 + 16 * 2560), pb[16], o0);
    o1 = mfma16k16(ltr(vb1 + 16 * st1), pb[16], o1);
    const float lsum = __builtin_bit_cast(float, __builtin_amdgcn_ds_bpermute((16 + col) * 4, __builtin_bit_cast(int, o1[0])));
    const float inv = 1.0f / lsum;
    const unsigned ob = (qw + col) * 1280 + (4 * hg + hh) * 40;
    gst64(rws, u2{pk2(o0[0] * inv, o0[1] * inv), pk2(o0[2] * inv, o0[3] * inv)}, ob + lg * 8, OFF_OA);
    if (lg == 0) gst64(rws, u2{pk2(o1[0] * inv, o1[1] * inv), pk2(o1[2] * inv, o1[3] * inv)}, ob + 32, OFF_OA);
  }
}

template <class Stored, class Ready>
__device__ __forceinline__ void task_tout(const Params& P, lptr sm, int seg, int cgp, Stored stored, Ready ready, unsigned off0, unsigned off1, unsigned lim0, unsigned lim1,
                                          volatile LAS unsigned* left) {
  const int tid = TID(), l = tid & 63, w = __builtin_amdgcn_readfirstlane(tid >> 6);
  const unsigned smb = (unsigned)(size_t)sm;
  const rsrc_t rws = mk_rsrc(P.ws, WS_USED), ro = mk_rsrc(P.out, 2048 * 2560);
  const int col = l & 15, lg = l >> 4;
  const int c0 = 16 * cgp + 2 * w;
  const unsigned vt = (col >> 3) * 16384 + lg * 256 + (col & 7) * 16, st = OFF_VOT + (seg * 80 + c0) * 16384;
  const unsigned vm = (col >> 3) * 4096 + lg * 256 + (col & 7) * 16, smo = OFF_VOM + (seg * 80 + c0) * 4096;
  u4 a[40];
  if (w == 0) stored();
  BAR();
#pragma unroll
  for (int k = 0; k < 32; ++k) a[k] = gld128(rws, vt, st + (k >> 1) * 1024 + (k & 1) * 128);
  SCHEDB();
  if (w == 0) ready();
  BAR();
#pragma unroll
  for (int y = 0; y < 10; ++y) {
    const int pc = w + 8 * y;
    if (y < 8) blds<true>(rws, (col >> 3) * 512 + lg * 128 + (col & 7) * 16, OFF_PT + seg * 81920 + (pc >> 1) * 2560 + (pc & 1) * 1024, smb + pc * 1024);
    else blds<true>(rws, (l >> 5) * 2560 + (l & 31) * 16, OFF_PT + seg * 81920 + (pc - 64) * 5120 + 2048, smb + pc * 1024);
  }
  const unsigned drawn = gld32(rws, w == 0 && l < 2 ? (l ? off1 : off0) : OOB, 0);
#pragma unroll
  for (int k = 32; k < 40; ++k) a[k] = gld128(rws, vm, smo + ((k - 32) >> 1) * 1024 + ((k - 32) & 1) * 128);
  WAITV(8);
  if (w == 0) { const bool some = __any(l < 2 && drawn + 1u < (l ? lim1 : lim0)); if (l == 0) *left = some; WAITL(0); }
  BAR();
  lptr bt = sm + l * 16;
  lptr bm = sm + 65536 + lg * 512 + col * 16;
  f4 acc[2] = {f4{0, 0, 0, 0}, f4{0, 0, 0, 0}};
  constexpr int PD = 8;
  h8 b[PD];
  auto rd = [&](int i) { const int k = i >> 1, tt = i & 1; return k < 32 ? lrd(bt + k * 2048 + tt * 1024) : lrd(bm + (k - 32) * 2048 + tt * 256); };
#pragma unroll
  for (int i = 0; i < PD - 1; ++i) b[i] = rd(i);
#pragma unroll
  for (int i = 0; i < 80; ++i) {
    if (i + PD - 1 < 80) b[(i + PD - 1) % PD] = rd(i + PD - 1);
    acc[i & 1] = mfma16(as_h8(a[i >> 1]), b[i % PD], acc[i & 1]);
    SCHEDB();
  }
  unsigned a0 = pk2(acc[0][0], acc[0][1]), a1 = pk2(acc[0][2], acc[0][3]), b0 = pk2(acc[1][0], acc[1][1]), b1 = pk2(acc[1][2], acc[1][3]);
  swap16u(a0, b0); swap16u(a1, b1);
  const int tok = 32 * seg + 16 * (lg & 1) + col;
  gst128(ro, u4{a0, a1, b0, b1}, tok * 2560 + 1280 + (128 * cgp + 16 * w + 8 * (lg >> 1)) * 2, 0);
}

constexpr int TT = 200, PHI = 136, PHM = 12;
constexpr int E0T = 96, E0P = 48;
constexpr int S1 = 10, DT1 = 8, DP1 = 4;
constexpr int RR = 96;
constexpr int LEAD = 120;
constexpr int EARLYN = 16;
constexpr int STAG = 40;
constexpr int NEED0 = 1;
constexpr int NECHO = 3, ECHOSL = 8;
constexpr int PSL = 16;
constexpr int NOUTW = 5;
constexpr unsigned CODEPF = 2, PFJ_LO = 2, PFJ_HI = 8;
constexpr int XKB(int k) { return 9 - k; }
__device__ __forceinline__ unsigned step_period(int s) { return s == 0 ? TT + E0T : s < S1 ? TT + DT1 : TT; }
__device__ __forceinline__ unsigned step_phase(int s) { return s == 0 ? PHI + E0P : s < S1 ? PHI + DP1 : PHI; }
constexpr int NSTEP = 64, NROLE = 112;
constexpr int SEG_LAST = 61;
constexpr int LASTOFS = 144;
constexpr unsigned SPIN_MAX = 1u << 14;
constexpr unsigned COPYB = 65536;
constexpr float CS_B = 0.08838834764831845f * 1.4426950408889634f;
__device__ __forceinline__ unsigned rt32() { unsigned long long t; asm volatile("s_memrealtime %0\n\ts_waitcnt lgkmcnt(0)" : "=s"(t) :: "memory"); return (unsigned)t; }
__device__ __forceinline__ void wait_until(unsigned target) {
  int n;
  const unsigned edge = __builtin_amdgcn_readfirstlane(target);
  asm volatile("1:\n\t"
               "s_memrealtime vcc\n\t"
               "s_waitcnt lgkmcnt(0)\n\t"
               "s_sub_i32 %0, %1, vcc_lo\n\t"
               "s_cmp_le_i32 %0, 0\n\t"
               "s_cbranch_scc1 4f\n\t"
               "s_cmp_le_i32 %0, 16\n\t"
               "s_cbranch_scc1 1b\n\t"
               "s_cmp_gt_i32 %0, 0x4000\n\t"
               "s_cbranch_scc1 4f\n\t"
               "s_sub_i32 %0, %0, 12\n\t"
               "s_mul_i32 %0, %0, 20\n\t"
               "s_lshr_b32 %0, %0, 6\n"
               "2:\n\t"
               "s_sleep 1\n\t"
               "s_sub_i32 %0, %0, 1\n\t"
               "s_cmp_gt_i32 %0, 0\n\t"
               "s_cbranch_scc1 2b\n\t"
               "s_branch 1b\n"
               "4:" : "=&s"(n) : "s"(edge) : "memory", "scc", "vcc");
}
__device__ __forceinline__ void rejoin(unsigned& base, unsigned ofs) {
  const int late = (int)(rt32() - (base + ofs));
  if (late > RR / 2 && late < (1 << 24)) base += (unsigned)((late + RR - 1) / RR) * RR;
}
__device__ __forceinline__ float fmax3(float a, float b, float c) { return __builtin_elementwise_maximum(__builtin_elementwise_maximum(a, b), c); }
__device__ __forceinline__ float fmax2(float a, float b) { return __builtin_elementwise_maximum(a, b); }
__device__ __forceinline__ bool any_nan4(f4 v) { return __any(__builtin_isunordered(v[0], v[1]) | __builtin_isunordered(v[2], v[3])); }

template <class Stored, class Ready>
__device__ __forceinline__ void task_tout_last(const Params& P, lptr sm, int seg, int cgp, int tt, Stored stored, Ready ready, unsigned off0, unsigned off1,
                                               unsigned lim0, unsigned lim1, volatile LAS unsigned* left, volatile LAS unsigned* fail) {
  const int tid = TID(), l = tid & 63, w = __builtin_amdgcn_readfirstlane(tid >> 6);
  const unsigned smb = (unsigned)(size_t)sm;
  const rsrc_t rws = mk_rsrc(P.ws, WS_USED), ro = mk_rsrc(P.out, 2048 * 2560);
  const int col = l & 15, lg = l >> 4;
  const int c0 = 16 * cgp + 2 * w;
  const unsigned vt = (col >> 3) * 16384 + lg * 256 + (col & 7) * 16, st = OFF_VOT + (seg * 80 + c0) * 16384;
  const unsigned vm = (col >> 3) * 4096 + lg * 256 + (col & 7) * 16, smo = OFF_VOM + (seg * 80 + c0) * 4096;
  const unsigned vout = (32 * seg + 16 * tt + col) * 2560 + 1280 + (128 * cgp + 16 * w + 4 * lg) * 2;
  u4 a[40];
  if (w == 0) stored();
  BAR();
#pragma unroll
  for (int k = 0; k < 32; ++k) a[k] = gld128(rws, vt, st + (k >> 1) * 1024 + (k & 1) * 128);
  SCHEDB();
  if (w == 0) ready();
  BAR();
  const unsigned drawn = gld32(rws, w == 0 && l < 2 ? (l ? off1 : off0) : OOB, 0);
  bool open = true;
  f4 t;
  for (int att = 0;; ++att) {
#pragma unroll
    for (int y = 0; y < 4; ++y)
      blds<true>(rws, (col >> 3) * 512 + lg * 128 + (col & 7) * 16, OFF_PT + seg * 81920 + (w + 8 * y) * 2560 + tt * 1024, smb + (w + 8 * y) * 1024);
    blds<true>(rws, lg * 2560 + col * 16, OFF_PT + seg * 81920 + w * 10240 + 2048 + tt * 256, smb + 32768 + w * 1024);
#pragma unroll
    for (int k = 32; k < 40; ++k) a[k] = gld128(rws, vm, smo + ((k - 32) >> 1) * 1024 + ((k - 32) & 1) * 128);
    WAITV(8);
    if (w == 0) { const bool some = __any(l < 2 && drawn + 1u < (l ? lim1 : lim0)); if (l == 0) *left = some; }
    BAR();
    f4 acc[2] = {f4{0, 0, 0, 0}, f4{0, 0, 0, 0}};
    constexpr int PD = 8;
    h8 b[PD];
#pragma unroll
    for (int i = 0; i < PD - 1; ++i) b[i] = lrd(sm + i * 1024 + l * 16);
#pragma unroll
    for (int i = 0; i < 40; ++i) {
      if (i + PD - 1 < 40) b[(i + PD - 1) % PD] = lrd(sm + (i + PD - 1) * 1024 + l * 16);
      acc[i & 1] = mfma16(as_h8(a[i]), b[i % PD], acc[i & 1]);
      SCHEDB();
    }
    t = acc[0] + acc[1];
    bool bad = any_nan4(t);
    if (!bad && open) { gst64(ro, u2{pk2(t[0], t[1]), pk2(t[2], t[3])}, vout, 0); open = false; }
    if (bad && att >= 3) {
      unsigned mx = 0;
#pragma unroll
      for (int k = 32; k < 40; ++k) mx = pkmax4(mx, a[k]);
#pragma unroll
      for (int y = 0; y < 5; ++y) mx = pkmax4(mx, *(const LAS u4*)(sm + (w + 8 * y) * 1024 + l * 16));
      bad = __any(unwritten(mx)) && att < (int)SPIN_MAX;
    }
    if (l == 0) fail[w] = bad;
    WAITL(0); BAR();
    if (!__any(fail[l & 7] != 0)) break;
  }
  if (open) gst64(ro, u2{pk2(t[0], t[1]), pk2(t[2], t[3])}, vout, 0);
}

__device__ __forceinline__ unsigned frag_rel(int f) { return (f >> 3) * 16384 + (f & 7) * 1024; }
struct Hand {
  unsigned base; unsigned par;
  __device__ __forceinline__ unsigned step(int s) const { return base + s * 131072 + ((par >> (s >> 4)) & 1) * 8192; }
};
__device__ __forceinline__ unsigned hand_base(const Params& P) { return (0x200000u - ((unsigned)(size_t)P.ws & 0x1FFFFFu)) & 0x1FFFFFu; }

template <int SLEEP = 4>
__device__ __forceinline__ unsigned poll_words(rsrc_t rws, unsigned off, int n, int l) {
  const unsigned vo = (l < n ? l : 0) * 4;
  unsigned v = SENT;
  for (int i = 0; i < POLL_MAX; ++i) {
    asm volatile("" ::: "memory");
    v = gld32(rws, vo, off);
    if (!__any(v == SENT)) break;
    __builtin_amdgcn_s_sleep(SLEEP);
  }
  return v;
}
__device__ __forceinline__ unsigned poll_par(rsrc_t rws, unsigned hb, int l) { return (unsigned)__ballot(poll_words(rws, hb + CTR_PAR, 4, l) == 1u) & 15u; }
__device__ __forceinline__ int poll_progress(rsrc_t rws, unsigned off, int n, int stride, int need, int l) {
  const unsigned vo = (l < n ? l : 0) * stride;
  unsigned v = 0;
  for (int i = 0; i < POLL_MAX; ++i) {
    asm volatile("" ::: "memory");
    v = gld32(rws, vo, off) + 1u;
    if (!__any(v < (unsigned)need)) break;
    __builtin_amdgcn_s_sleep(8);
  }
  unsigned m = 0;
  for (unsigned b = 64; b; b >>= 1) if (!__any(v < m + b)) m += b;
  return (int)m;
}
__device__ __forceinline__ unsigned prog_word(unsigned count, unsigned base) { return (base >> 2) << 8 | count; }
template <int SLEEP>
__device__ __forceinline__ unsigned poll_above(rsrc_t rws, unsigned off, unsigned v) {
  unsigned x = 0;
  for (int i = 0; i < POLL_MAX; ++i) {
    asm volatile("" ::: "memory");
    x = __builtin_amdgcn_readfirstlane(gld32(rws, 0, off));
    if (((x + 1u) & 255u) > v + 1u) break;
    __builtin_amdgcn_s_sleep(SLEEP);
  }
  return x;
}
__device__ __forceinline__ void wait_phase(unsigned word, unsigned ofs) {
  const unsigned now = rt32();
  const int d = (int)((((word >> 8) << 2) + ofs - now) << 6) >> 6;
  if (d > 0 && d < 4 * TT) wait_until(now + d);
}
__device__ __forceinline__ unsigned count_ready(const Params& P, const Hand& H) {
  unsigned old = 0;
  if ((threadIdx.x & 63) == 0) old = __hip_atomic_fetch_add((unsigned*)(P.ws + H.base + CTR_READY), 1u, __ATOMIC_RELAXED, __HIP_MEMORY_SCOPE_AGENT);
  return old;
}
__device__ __forceinline__ unsigned start_sync(const Params& P, const Hand& H, unsigned old) {
  unsigned* start = (unsigned*)(P.ws + H.base + CTR_START);
  unsigned v = SENT;
  if ((threadIdx.x & 63) == 0) {
    if (old == NROLE - 2) { v = (rt32() + LEAD) & ~3u; __hip_atomic_store(start, v, __ATOMIC_RELAXED, __HIP_MEMORY_SCOPE_AGENT); }
    for (int i = 0; i < (1 << 20) && v == SENT; ++i) { __builtin_amdgcn_s_sleep(2); v = __hip_atomic_load(start, __ATOMIC_RELAXED, __HIP_MEMORY_SCOPE_AGENT); }
  }
  return __builtin_amdgcn_readfirstlane(v);
}

__device__ __forceinline__ void gather10(rsrc_t r, unsigned voff, unsigned s1, unsigned s2, unsigned s3, unsigned lds) {
  asm volatile("s_mov_b32 m0, %5\n\ts_nop 0\n\t"
               "buffer_load_dwordx4 %0, %1, %2 offen sc1 lds\n\t"
               "buffer_load_dwordx4 %0, %1, %2 offen offset:1024 sc1 lds\n\t"
               "buffer_load_dwordx4 %0, %1, %2 offen offset:2048 sc1 lds\n\t"
               "buffer_load_dwordx4 %0, %1, %2 offen offset:3072 sc1 lds\n\t"
               "s_add_u32 m0, m0, 0x1000\n\ts_nop 0\n\t"
               "buffer_load_dwordx4 %0, %1, %3 offen sc1 lds\n\t"
               "buffer_load_dwordx4 %0, %1, %3 offen offset:1024 sc1 lds\n\t"
               "buffer_load_dwordx4 %0, %1, %3 offen offset:2048 sc1 lds\n\t"
               "buffer_load_dwordx4 %0, %1, %3 offen offset:3072 sc1 lds\n\t"
               "s_add_u32 m0, m0, 0x1000\n\ts_nop 0\n\t"
               "buffer_load_dwordx4 %0, %1, %4 offen sc1 lds\n\t"
               "buffer_load_dwordx4 %0, %1, %4 offen offset:1024 sc1 lds"
               :: "v"(voff), "s"(r), "s"(s1), "s"(s2), "s"(s3), "s"(lds) : "memory", "scc");
}
__device__ __forceinline__ void load4(rsrc_t r, unsigned voff, unsigned s1, unsigned lds) {
  asm volatile("s_mov_b32 m0, %3\n\ts_nop 0\n\t"
               "buffer_load_dwordx4 %0, %1, %2 offen sc1 lds\n\t"
               "buffer_load_dwordx4 %0, %1, %2 offen offset:1024 sc1 lds\n\t"
               "buffer_load_dwordx4 %0, %1, %2 offen offset:2048 sc1 lds\n\t"
               "buffer_load_dwordx4 %0, %1, %2 offen offset:3072 sc1 lds"
               :: "v"(voff), "s"(r), "s"(s1), "s"(lds) : "memory");
}
__device__ __forceinline__ void load2(rsrc_t r, unsigned voff, unsigned s1, unsigned lds) {
  asm volatile("s_mov_b32 m0, %3\n\ts_nop 0\n\t"
               "buffer_load_dwordx4 %0, %1, %2 offen sc1 lds\n\t"
               "buffer_load_dwordx4 %0, %1, %2 offen offset:1024 sc1 lds"
               :: "v"(voff), "s"(r), "s"(s1), "s"(lds) : "memory");
}
template <bool GATHER, class WF, class Wait1, class Early, class Mid, class Post, class Wait2, class Pre>
__device__ __forceinline__ void gather_project(lptr sm, unsigned memoff, rsrc_t rws, unsigned stepoff, int l, int idx, WF wf,
                                               unsigned& budget, unsigned& base, unsigned ofs, Wait1 wait1, Early early, Mid mid, Post post, Wait2 wait2, Pre pre) {
  const unsigned smb = (unsigned)(size_t)sm + memoff;
  int att = 0;
  for (bool first = true;; first = false) {
    if (GATHER) {
      rejoin(base, ofs);
      unsigned s1 = stepoff + frag_rel(20), s2 = stepoff + frag_rel(24), s3 = stepoff + frag_rel(28), tg = base + ofs, vo = l * 16;
      asm volatile("" : "+s"(s1), "+s"(s2), "+s"(s3), "+s"(tg), "+v"(vo));
      wait_until(tg);
      gather10(rws, vo, s1, s2, s3, smb);
      WAITV(0);
    } else wait1();
    BAR();
    if (first) early();
    h8 a[10];
#pragma unroll
    for (int kb = 0; kb < 5; ++kb) a[kb] = lrd(sm + memoff + kb * 1024 + l * 16);
    SCHEDB();
    f4 d0 = f4{0, 0, 0, 0}, d1 = f4{0, 0, 0, 0};
#pragma unroll
    for (int kb = 0; kb < 10; ++kb) {
      if (kb + 5 < 10) a[kb + 5] = lrd(sm + memoff + (kb + 5) * 1024 + l * 16);
      if (kb == 9) { asm volatile("" : "+v"(a[9])); mid(); }
      d0 = smfma(a[kb], wf(0, kb), d0, idx);
      d1 = smfma(a[kb], wf(1, kb), d1, idx);
      SCHEDB();
    }
#pragma unroll
    for (int r = 0; r < 4; ++r) { float x = d0[r], y = d1[r]; swap32(x, y); d0[r] = x + y; }
    post(d0);
    wait2();
    WAITL(0); BAR();
    pre();
    SCHEDB();
    if (!any_nan4(d0) || budget == 0) break;
    if (++att >= 3) {
      unsigned mx = 0;
#pragma unroll
      for (int kb = 0; kb < 10; ++kb) mx = pkmax4(mx, *(const LAS u4*)(sm + memoff + kb * 1024 + l * 16));
      if (!__any(unwritten(mx))) break;
    }
    --budget;
    base += RR;
    stepoff ^= COPYB;
    WAITL(0); BAR();
  }
}

__device__ __forceinline__ void softmax40(f4 t0, f4 t1, f4 mm, int lg, u4& ptok, u2& pmem) {
  if (lg < 2) mm = f4{NEG_INF, NEG_INF, NEG_INF, NEG_INF};
  float m = fmax3(fmax3(t0[0], t0[1], t0[2]), fmax3(t0[3], t1[0], t1[1]), fmax3(t1[2], t1[3], mm[0]));
  m = fmax2(m, fmax3(mm[1], mm[2], mm[3]));
  m = xmax4(m);
  const float mc = m * CS_B;
  float sum = 0;
#pragma unroll
  for (int r = 0; r < 4; ++r) {
    t0[r] = __builtin_amdgcn_exp2f(__builtin_fmaf(t0[r], CS_B, -mc));
    t1[r] = __builtin_amdgcn_exp2f(__builtin_fmaf(t1[r], CS_B, -mc));
    mm[r] = __builtin_amdgcn_exp2f(__builtin_fmaf(mm[r], CS_B, -mc));
    sum += t0[r] + t1[r] + mm[r];
  }
  const float inv = __builtin_amdgcn_rcpf(xsum4(sum));
  ptok = u4{nos(pk2(t0[0] * inv, t0[1] * inv)), nos(pk2(t0[2] * inv, t0[3] * inv)), nos(pk2(t1[0] * inv, t1[1] * inv)), nos(pk2(t1[2] * inv, t1[3] * inv))};
  pmem = u2{nos(pk2(mm[0] * inv, mm[1] * inv)), nos(pk2(mm[2] * inv, mm[3] * inv))};
}
__device__ __forceinline__ float dpp_ror8(float x) {
  return __builtin_bit_cast(float, __builtin_amdgcn_update_dpp(0, __builtin_bit_cast(int, x), 0x128, 0xF, 0xF, true));
}
__device__ __forceinline__ unsigned dpp_ror8u(unsigned x) { return (unsigned)__builtin_amdgcn_update_dpp(0, (int)x, 0x128, 0xF, 0xF, true); }
__device__ __forceinline__ void softmax40_dup(f4 t0, f4 t1, f4 mm, int l, u4& ptok, unsigned& pmem) {
  const bool hi = (l & 8) != 0;
  float m = fmax2(fmax3(fmax3(t0[0], t0[1], t0[2]), fmax3(t0[3], t1[0], t1[1]), fmax3(t1[2], t1[3], mm[0])), fmax3(mm[1], mm[2], mm[3]));
  float t[4];
#pragma unroll
  for (int r = 0; r < 4; ++r) t[r] = hi ? t1[r] : t0[r];
  const float ma = hi ? mm[2] : mm[0], mb = hi ? mm[3] : mm[1];
  float x = l >= 32 ? mb : ma;
  m = xmax4(m);
  const float mc = m * CS_B;
#pragma unroll
  for (int r = 0; r < 4; ++r) t[r] = __builtin_amdgcn_exp2f(__builtin_fmaf(t[r], CS_B, -mc));
  x = __builtin_amdgcn_exp2f(__builtin_fmaf(x, CS_B, -mc));
  float sum = (t[0] + t[1]) + (t[2] + t[3]) + x;
  sum += dpp_ror8(sum);
  const float inv = __builtin_amdgcn_rcpf(xsum4(sum));
  const unsigned p0 = nos(pk2(t[0] * inv, t[1] * inv)), p1 = nos(pk2(t[2] * inv, t[3] * inv));
  ptok = u4{p0, p1, dpp_ror8u(p0), dpp_ror8u(p1)};
  pmem = nos(pk2(x * inv, x * inv));
}

constexpr int XL_MEM = 0, XL_T = 10240, XL_DUM = 14336, XL_KQ = 16384;
__device__ __forceinline__ bool role_x(const Params& P, lptr sm, int h, unsigned hb, Hs& hs) {
  const int tid = TID(), l = tid & 63, w = __builtin_amdgcn_readfirstlane(tid >> 6);
  const unsigned smb = (unsigned)(size_t)sm;
  const rsrc_t rws = mk_rsrc(P.ws, WS_USED), rq = mk_rsrc(P.Wq, 640 * 8192), rk = mk_rsrc(P.Wk, 640 * 8192);
  const int g = l >> 4, q = (l & 15) >> 2, p = l & 3, col = l & 15;
  h16 wf[2][10];
  u2 ev = u2{0, 0};
  {
    auto rho = [](int row) { return ((row >> 3) & 1) * 4 + ((row >> 2) & 1) * 2 + (row & 1); };
    auto stage = [&](int kb) {
#pragma unroll
      for (int y = 0; y < 2; ++y) {
        const int pc = w + 8 * y, row = 4 * pc + (l >> 4), un = l & 15;
        const unsigned vo = (row & 7) * 8192 + h * 256 + (((un >> 1) ^ rho(row)) * 32) + (un & 1) * 16;
        const unsigned so = mem_group(8 * XKB(kb) + (pc >> 1)) * (8 * 8192);
        blds<false>(rq, vo, so, smb + (kb & 3) * 32768 + pc * 1024);
        blds<false>(rk, vo, so, smb + (kb & 3) * 32768 + 16384 + pc * 1024);
      }
    };
    stage(0); stage(1); stage(2);
#pragma unroll
    for (int kb = 0; kb < 10; ++kb) {
      if (kb < 8) WAITV(8); else if (kb == 8) WAITV(4); else WAITV(0);
      BAR();
      if (kb + 3 < 10) stage(kb + 3);
      if (kb == 7 && w == 0) ev = hs.peek();
#pragma unroll
      for (int ty = 0; ty < 2; ++ty) {
        h4 v[4];
#pragma unroll
        for (int u = 0; u < 4; ++u) {
          const int row = 16 * (2 * (u >> 1) + (g >> 1)) + 8 * ((q >> 1) & 1) + 4 * (g & 1) + 2 * (u & 1) + (q & 1);
          v[u] = ltr(sm + (kb & 3) * 32768 + (w >> 2) * 16384 + row * 256 + (((2 * (w & 3) + ty) ^ rho(row)) * 32) + p * 8);
        }
        const h8 lo = cat8(v[0], v[1]), hi = cat8(v[2], v[3]);
        wf[ty][XKB(kb)] = __builtin_shufflevector(lo, hi, 0, 1, 2, 3, 4, 5, 6, 7, 8, 9, 10, 11, 12, 13, 14, 15);
      }
      WAITL(0);
    }
    BAR();
  }
  if (!hs.check(sm, ev)) return false;
  const int idx = (l & 8) ? 0xEEEE : 0x4444;
  Hand H; H.base = hb; H.par = poll_par(rws, hb, l);
  auto prefetch = [&](int s) {
    load4(rws, l * 16, (w < 6 ? OFF_KT : OFF_QT) + (s * 32 + h) * 8192 + (w & 1) * 4096, smb + XL_KQ + (s & 1) * 16384 + (w - 4) * 4096);
  };
  const unsigned vtouch = l < 32 ? frag_rel(h >> 1) + ((l >> 3) * 16 + (h & 1) * 8 + (l & 7)) * 16 : frag_rel(16 + (h >> 3)) + ((h & 3) * 16 + ((h >> 2) & 1) * 8 + (l & 7)) * 16;
  auto touch = [&](int s) {
    blds<true>(rws, vtouch, H.step(s), smb + XL_DUM);
    blds<true>(rws, vtouch, H.step(s) + COPYB, smb + XL_DUM);
  };
  {
    unsigned sent = SENT; asm volatile("" : "+v"(sent));
    const unsigned vfill = l < 40 ? vtouch : OOB;
#pragma unroll 4
    for (int y = 0; y < 16; ++y) gst128(rws, u4{sent, sent, sent, sent}, vfill, H.step(8 * w + (y >> 1)) + (y & 1) * COPYB);
    const int sg = tid / 160;
    gst128(rws, u4{sent, sent, sent, sent}, tid < 480 ? sg * 81920 + (tid - 160 * sg) * 16 : OOB, OFF_PT + (SEG_LAST * 32 + h) * 2560);
  }
  unsigned budget = SPIN_MAX, base = 0;
  int prog = 0;
  if (w >= 4) { prog = poll_progress(rws, hb + PR_QK + h * 128, 1, 0, NEED0, l); prefetch(0); }
  if (w == 3) touch(0);
  WAITV(0); BAR();
  if (w >= 4 && prog < 2) prog = poll_progress(rws, hb + PR_QK + h * 128, 1, 0, 2, l);
  if (w == 3) base = start_sync(P, H, count_ready(P, H));
  auto wff = [&](int t, int kb) { return wf[t][kb]; };
  auto post = [&](f4 d) { *(LAS u2*)(sm + XL_T + (32 * (w & 3) + 16 * (l >> 5) + col) * 32 + (w >> 2) * 16 + (g & 1) * 8) = u2{pk2(d[0], d[1]), pk2(d[2], d[3])}; };
  auto nop = []() {};
  auto drain = []() { WAITV(0); };
  if (w == 0) {
    const unsigned vptok = col < 8 ? (g * 16 + (h & 1) * 8 + col) * 16 + frag_rel(h >> 1) : OOB;
    const unsigned vpmem = ((h & 3) * 16 + ((h >> 2) & 1) * 8 + (col & 7)) * 16 + (4 * (g & 1) + 2 * (col >> 3) + (g >> 1)) * 2 + frag_rel(16 + (h >> 3));
    for (int s = 0; s < NSTEP; ++s) {
      unsigned so = H.step(s), so2 = so + COPYB;
      asm volatile("" : "+s"(so), "+s"(so2));
      lptr kq = sm + XL_KQ + (s & 1) * 16384;
      h8 kt[2][4], K[4], Q[4];
      gather_project<false>(sm, XL_MEM, rws, so, l, idx, wff, budget, base, 0u, nop, nop, [&]() {
#pragma unroll
        for (int m = 0; m < 4; ++m) { kt[0][m] = lrd(kq + m * 1024 + l * 16); kt[1][m] = lrd(kq + (4 + m) * 1024 + l * 16); }
      }, post, nop, [&]() {
#pragma unroll
        for (int m = 0; m < 4; ++m) {
          lptr tb = sm + XL_T + (32 * m + 4 * g + q) * 32 + (p & 1) * 8;
          Q[m] = cat8(ltr(tb), ltr(tb + 512));
          K[m] = cat8(ltr(tb + 16), ltr(tb + 528));
        }
      });
      f4 mm = {0, 0, 0, 0}, t0 = {0, 0, 0, 0}, t1 = {0, 0, 0, 0};
#pragma unroll
      for (int m = 0; m < 4; ++m) {
        t0 = mfma16(kt[0][m], Q[m], t0);
        t1 = mfma16(kt[1][m], Q[m], t1);
        mm = mfma16(K[m], Q[m], mm);
        SCHEDB();
      }
      u4 pt; unsigned pm;
      softmax40_dup(t0, t1, mm, l, pt, pm);
      gst128(rws, pt, vptok, so);
      __builtin_amdgcn_raw_buffer_store_b16((unsigned short)pm, rws, vpmem, so, 16);
      SCHEDB();
      gst128(rws, pt, vptok, so2);
      __builtin_amdgcn_raw_buffer_store_b16((unsigned short)pm, rws, vpmem, so2, 16);
    }
  } else if (w < 3) {
    const int tq = 8 * (col >> 2) + (col & 3) + 4 * (w - 1);
    const unsigned vt = (((tq >> 3) * 4 + g) * 8 + (tq & 7)) * 16, vm = g >= 2 ? 2048 + tq * 16 + (g - 2) * 8 : OOB;
    for (int s = 0; s < NSTEP; ++s) {
      lptr kq = sm + XL_KQ + (s & 1) * 16384;
      h8 kt[2][4];
      gather_project<false>(sm, XL_MEM, rws, 0u, l, idx, wff, budget, base, 0u, drain, nop, [&]() {
#pragma unroll
        for (int m = 0; m < 4; ++m) { kt[0][m] = lrd(kq + m * 1024 + l * 16); kt[1][m] = lrd(kq + (4 + m) * 1024 + l * 16); }
      }, post, nop, nop);
      f4 mm = {0, 0, 0, 0}, t0 = {0, 0, 0, 0}, t1 = {0, 0, 0, 0};
#pragma unroll
      for (int m = 0; m < 4; ++m) {
        lptr tb = sm + XL_T + (32 * m + 4 * g + q) * 32 + p * 8;
        const h8 F = cat8(ltr(tb), ltr(tb + 512));
        const h8 B = lrd(kq + 8192 + ((w - 1) * 4 + m) * 1024 + l * 16);
        mm = mfma16(F, B, mm);
        t0 = mfma16(kt[0][m], B, t0);
        t1 = mfma16(kt[1][m], B, t1);
      }
      u4 pt; u2 pm;
#pragma unroll
      for (int r = 0; r < 4; ++r) {
        if (8 * g + r > tq) t0[r] = NEG_INF;
        if (8 * g + 4 + r > tq) t1[r] = NEG_INF;
      }
      softmax40(t0, t1, mm, g, pt, pm);
      const unsigned pto = OFF_PT + (s * 32 + h) * 2560;
      gst128(rws, pt, vt, pto);
      gst64(rws, pm, vm, pto);
    }
  } else if (w == 3) {
    for (int s = 0; s < NSTEP; ++s) {
      gather_project<true>(sm, XL_MEM, rws, H.step(s), l, idx, wff, budget, base, 0u, nop, nop, nop, post, nop, nop);
      if (h == 0 && s >= 3) gst32(rws, prog_word(s - 2, base), l == 0 ? 0 : OOB, hb + CTR_PROG);
      if (s + 1 < NSTEP) touch(s + 1);
      if (s + 1 < NSTEP) {
        wait_until(base + step_phase(s) - (s == 0 ? E0P : 0) - 8);
        load4(rws, l * 16, OFF_QT + ((s + 1) * 32 + h) * 8192, smb + XL_KQ + ((s + 1) & 1) * 16384 + 8192);
        load4(rws, l * 16, OFF_QT + ((s + 1) * 32 + h) * 8192 + 4096, smb + XL_KQ + ((s + 1) & 1) * 16384 + 12288);
      }
      base += step_period(s);
    }
  } else {
    for (int s = 0; s < NSTEP; ++s) {
      gather_project<false>(sm, XL_MEM, rws, 0u, l, idx, wff, budget, base, 0u, drain, [&]() {
        if (s + 1 < NSTEP && w < 6) prefetch(s + 1);
      }, nop, post, nop, nop);
      if (w == 4 && s + 2 < NSTEP && prog < s + 3) prog = poll_progress(rws, hb + PR_QK + h * 128, 1, 0, s + 3, l);
    }
  }
  WAITV(0); BAR();
  return true;
}

constexpr int ZL_MEM = 0, ZL_DUM = 10240, ZL_BASE = 11264, ZL_AV = 16384, ZL_AVB = 20480, ZL_W = ZL_AV + 2 * ZL_AVB;
constexpr int NPROBE = 20;
constexpr int PROBE_C = 32;
__device__ __forceinline__ unsigned probe_window(const Params& P, unsigned hb, int x) {
  const unsigned xcc = __builtin_amdgcn_s_getreg(20 | (3 << 11)) & 15;
  unsigned best[2] = {~0u, ~0u};
#pragma unroll 1
  for (int r = 0; r < NPROBE; ++r) {
    if (r >= 4 && (4 * best[0] < 3 * best[1] || 4 * best[1] < 3 * best[0])) break;
#pragma unroll
    for (int pq = 0; pq < 2; ++pq) {
      const char* a = P.ws + hb + x * 0x200000 + (3 + (r & 7)) * 131072 + 3 * 16384 + pq * 8192 + 6 * 1024 + (r & 3) * 256;
      unsigned long long c0, c1; unsigned v;
      asm volatile("s_memtime %0\n\ts_waitcnt lgkmcnt(0)\n\tglobal_load_dword %2, %3, off sc1\n\ts_waitcnt vmcnt(0)\n\ts_memtime %1\n\ts_waitcnt lgkmcnt(0)"
                   : "=&s"(c0), "=&s"(c1), "=&v"(v) : "v"(a) : "memory");
      const unsigned d = (unsigned)(c1 - c0);
      best[pq] = d < best[pq] ? d : best[pq];
    }
  }
  return (best[1] < best[0] ? 1u : 0u) ^ (xcc >= 4 ? 1u : 0u);
}
__device__ __forceinline__ bool role_z(const Params& P, lptr sm, int c, unsigned hb, Hs& hs) {
  const int tid = TID(), l = tid & 63, w = __builtin_amdgcn_readfirstlane(tid >> 6);
  const unsigned smb = (unsigned)(size_t)sm;
  const rsrc_t rws = mk_rsrc(P.ws, WS_USED);
  const int idx = (l & 8) ? 0xEEEE : 0x4444;
  volatile LAS unsigned* lbase = (volatile LAS unsigned*)(sm + ZL_BASE);
  const bool prober = c >= PROBE_C && c < PROBE_C + 4;
  if (prober && w == 0) { const unsigned near = probe_window(P, hb, c - PROBE_C); if (l == 0) *lbase = near; WAITL(0); }
  if (!hs.ok) {
    const u2 ev = w == 0 ? hs.peek() : u2{0, 0};
    WAITV(0);
    if (!hs.check(sm, ev)) return false;
  }
  if (prober) {
    BAR();
    const unsigned near = __builtin_amdgcn_readfirstlane(*lbase);
    if (c == PROBE_C) {
      for (int i = tid; i < 640; i += 512) {
        const int cc = i >> 3, r = i & 7, n = mem_line(cc);
        const u4 v = *(const u4*)(P.Bmem0 + r * 1280 + cc * 16);
        const u4 o = u4{nos(v[0]), nos(v[1]), nos(v[2]), nos(v[3])};
        const unsigned d = hb + near * 8192 + frag_rel(20 + (n >> 3)) + (n & 7) * 128 + r * 16;
        gst128(rws, o, d, 0); gst128(rws, o, d, COPYB);
      }
    }
    if (w == 0) gst32(rws, near, l == 0 ? 0 : OOB, hb + CTR_PAR + (c - PROBE_C) * 4);
    BAR();
  }
  Hand H; H.base = hb; H.par = poll_par(rws, hb, l);
  const unsigned mline = frag_rel(20 + (mem_line(c) >> 3)) + (mem_line(c) & 7) * 128;
  {
    unsigned sent = SENT; asm volatile("" : "+v"(sent));
#pragma unroll
    for (int y = 0; y < 2; ++y) {
      const int s = 8 * w + 4 * y + (l >> 4);
      gst128(rws, u4{sent, sent, sent, sent}, s > 0 ? s * 131072 + ((l >> 3) & 1) * COPYB + (l & 7) * 16 : OOB, H.step(8 * w) - 8 * w * 131072 + mline);
    }
#pragma unroll
    for (int y = 0; y < 2; ++y) {
      const int i = tid + 512 * y;
      gst128(rws, u4{sent, sent, sent, sent}, i < 768 ? (i >> 8) * (80 * 4096) + (i & 255) * 16 : OOB, OFF_VOM + (SEG_LAST * 80 + c) * 4096);
    }
  }
  if (w == 0) { const unsigned fl = hb + FL_FOLD + (c >> 4) * 512; poll_words<8>(rws, fl, 64, l); poll_words<8>(rws, fl + 256, 32, l); }
  WAITV(0); BAR();
  const int rot = 5 * ((c >> 2) & 3) + (c >> 4);
  auto slot = [&](int f) { const int t = f - rot; return (t < 0 ? t + 20 : t) * 1024; };
  auto prefetch = [&](int s) {
#pragma unroll
    for (int y = 0; y < 4; ++y) {
      const int f = (w & 3) * 4 + y;
      blds<true>(rws, l * 16, OFF_VOT + (s * 80 + c) * 16384 + f * 1024, smb + ZL_AV + (s & 1) * ZL_AVB + slot(f));
    }
  };
  auto nop = []() {};
  const unsigned vwl = ((l >> 3) & 1) * 1024 + (l >> 4) * 128 + (l & 7) * 16;
  const int vpost = ZL_AV + slot(16 + (w >> 1)) + ((2 * (l >> 5) + ((l >> 3) & 1)) * 16 + (w & 1) * 8 + (l & 7)) * 16 + ((l >> 4) & 1) * 8;
  auto post = [&](int s) { return [&, s](f4 d) { *(LAS u2*)(sm + vpost + (s & 1) * ZL_AVB) = u2{pk2(d[0], d[1]), pk2(d[2], d[3])}; }; };
  if (w == 0) {
#pragma unroll 4
    for (int y = 0; y < 40; ++y) blds<true>(rws, vwl, OFF_WVO + c * 327680 + (y >> 1) * 2048 + (y & 1) * 512, smb + ZL_W + y * 1024);
    WAITV(EARLYN);
    const unsigned ticket = count_ready(P, H);
    WAITV(0); BAR();
    unsigned vp[20];
#pragma unroll
    for (int t = 0; t < 20; ++t) { const int f = t + rot >= 20 ? t + rot - 20 : t + rot; vp[t] = l * 16 + frag_rel(f); asm volatile("" : "+v"(vp[t])); }
    unsigned budget = SPIN_MAX, budget2 = SPIN_MAX;
    const unsigned vpub = l < 32 && (l & 8) == 0 ? (l & 7) * 16 + (l >> 4) * 8 : OOB;
    unsigned base = start_sync(P, H, ticket);
    for (int s = 0; s < NSTEP; ++s) {
      *lbase = base;
      WAITL(0); BAR();
      const unsigned so = H.step(s);
      h16 wf[2][10];
#pragma unroll
      for (int t = 0; t < 2; ++t)
#pragma unroll
        for (int kb = 0; kb < 10; ++kb) {
          lptr b = sm + ZL_W + (t * 10 + kb) * 2048 + l * 16;
          wf[t][kb] = __builtin_shufflevector(lrd(b), lrd(b + 1024), 0, 1, 2, 3, 4, 5, 6, 7, 8, 9, 10, 11, 12, 13, 14, 15);
        }
      WAITL(0);
      gather_project<false>(sm, ZL_MEM, rws, so, l, idx, [&](int t, int kb) { return wf[t][kb]; }, budget, base, (unsigned)PHM, nop, nop, nop, post(s), nop, nop);
      if (s + 1 == NSTEP) break;
      h8 av[20];
#pragma unroll
      for (int t = 0; t < 20; ++t) av[t] = lrd(sm + ZL_AV + (s & 1) * ZL_AVB + t * 1024 + l * 16);
      const unsigned son = H.step(s + 1) + mline;
      WAITL(0);
#pragma unroll
      for (int t = 0; t < 20; ++t) asm volatile("" : "+v"(av[t]));
      f4 a0, a1;
      unsigned vo = so;
      int att = 0;
      const unsigned phi = step_phase(s);
      for (;;) {
        u4 pb[20];
        rejoin(base, phi);
        wait_until(base + phi);
#pragma unroll
        for (int t = 0; t < 20; ++t) pb[t] = gld128(rws, vp[t], vo);
        a0 = f4{0, 0, 0, 0}; a1 = f4{0, 0, 0, 0};
        SCHEDB();
#pragma unroll
        for (int t = 0; t < 20; ++t) {
          if (t & 1) a1 = mfma16(av[t], as_h8(pb[t]), a1); else a0 = mfma16(av[t], as_h8(pb[t]), a0);
        }
        a0 += a1;
        const float chk = a0[0];
        if (!__any(chk != chk) || budget2 == 0) break;
        if (++att >= 3) {
          unsigned mx = 0;
#pragma unroll
          for (int t = 0; t < 20; ++t) mx = pkmax4(mx, pb[t]);
          if (!__any(unwritten(mx))) break;
        }
        --budget2;
        base += RR;
        vo ^= COPYB;
      }
      float o[4];
#pragma unroll
      for (int r = 0; r < 4; ++r) {
        float x = a0[r], y = x;
        swap32(x, y);
        o[r] = a0[r] + dpp_ror8(y);
      }
      {
        const u2 x = u2{nos(pk2(o[0], o[1])), nos(pk2(o[2], o[3]))};
        gst64(rws, x, vpub, son);
        SCHEDB();
        gst64(rws, x, vpub, son + COPYB);
      }
      base += step_period(s);
    }
  } else {
    h16 wf[2][10];
#pragma unroll
    for (int t = 0; t < 2; ++t)
#pragma unroll
      for (int kb = 0; kb < 10; ++kb) {
        const unsigned so = OFF_WVO + c * 327680 + ((2 * w + t) * 10 + kb) * 2048;
        wf[t][kb] = __builtin_shufflevector(as_h8(gld128(rws, vwl, so)), as_h8(gld128(rws, vwl, so + 512)), 0, 1, 2, 3, 4, 5, 6, 7, 8, 9, 10, 11, 12, 13, 14, 15);
      }
    int prog = 0;
    if (w >= 4) {
      if (w == 4) WAITV(0);
      prog = poll_progress(rws, hb + PR_VO, 32, 4, NEED0, l); prefetch(0);
    }
    const unsigned vtouch = mline + (l & 7) * 16 + (l >> 5) * COPYB;
    auto touch = [&](int s) { blds<true>(rws, vtouch, H.step(s), smb + ZL_DUM); };
    if (w == 3) touch(1);
    unsigned budget = SPIN_MAX;
    auto wff = [&](int t, int kb) { return wf[t][kb]; };
    WAITV(0); BAR();
    if (w >= 4 && prog < 2) prog = poll_progress(rws, hb + PR_VO, 32, 4, 2, l);
    if (w == 1) {
      for (int s = 0; s < NSTEP; ++s) {
        BAR();
        unsigned base = __builtin_amdgcn_readfirstlane(*lbase);
        gather_project<true>(sm, ZL_MEM, rws, H.step(s), l, idx, wff, budget, base, (unsigned)PHM, nop, nop, nop, post(s), nop, nop);
      }
    } else {
      const unsigned vvom = w >= 2 && w < 6 ? (w - 2) * 1024 + l * 16 : OOB;
      u4 vom = u4{0, 0, 0, 0};
      for (int s = 0; s < NSTEP; ++s) {
        BAR();
        unsigned base = 0;
        gather_project<false>(sm, ZL_MEM, rws, 0u, l, idx, wff, budget, base, (unsigned)PHM, nop, [&]() {
          if (w >= 4 && s + 1 < NSTEP) prefetch(s + 1);
          if (s > 0) gst128(rws, vom, vvom, OFF_VOM + ((s - 1) * 80 + c) * 4096);
          if (w == 3 && s + 2 < NSTEP) touch(s + 2);
        }, nop, post(s), [&]() {
          if (s == 0 || s + 2 >= NSTEP) WAITV(0);
          else if (w >= 4) WAITV(5);
          else if (w == 3) WAITV(2);
          else WAITV(1);
        }, nop);
        if (w == 4 && s + 2 < NSTEP && prog < s + 3) prog = poll_progress(rws, hb + PR_VO, 32, 4, s + 3, l);
        vom = *(const LAS u4*)(sm + ZL_AV + (s & 1) * ZL_AVB + slot(16 + ((w - 2) & 3)) + l * 16);
        vom = u4{nos(vom[0]), nos(vom[1]), nos(vom[2]), nos(vom[3])};
        asm volatile("" : "+v"(vom));
      }
      gst128(rws, vom, vvom, OFF_VOM + ((NSTEP - 1) * 80 + c) * 4096);
    }
  }
  WAITV(0); BAR();
  return true;
}

constexpr int A2_FIRST = 80, A2_LAST = 240;
constexpr int A2DEF = 56;
constexpr int NQ = 240;
__device__ const unsigned short kOrder[NQ] = {
  8192, 8193, 8194, 8195, 8196, 8197, 8198, 8199, 8200, 8201, 8202, 8203, 8204, 8205, 8206, 8207, 8208, 8209, 8210, 8211, 8212, 8213, 8214, 8215,
  8216, 8217, 8218, 8219, 8220, 8221, 8222, 8223, 8224, 8225, 8226, 8227, 8228, 8229, 8230, 8231, 8232, 8233, 8234, 8235, 8236, 8237, 8238, 8239,
  8240, 8241, 8242, 8243, 8244, 8245, 8246, 8247, 8248, 8249, 8250, 8251, 8252, 8253, 8254, 8255, 8256, 8257, 8258, 8259, 8260, 8261, 8262, 8263,
  8264, 8265, 8266, 8267, 8268, 8269, 8270, 8271, 12288, 12289, 12290, 12291, 12292, 12293, 12294, 12295, 12296, 12297, 12298, 12299, 12300, 12301, 12302, 12303,
  12304, 12305, 12306, 12307, 12308, 12309, 12310, 12311, 12312, 12313, 12314, 12315, 12316, 12317, 12318, 12319, 12320, 12321, 12322, 12323, 12324, 12325, 12326, 12327,
  12328, 12329, 12330, 12331, 12332, 12333, 12334, 12335, 12336, 12337, 12338, 12339, 12340, 12341, 12342, 12343, 12344, 12345, 12346, 12347, 12348, 12349, 12350, 12351,
  12352, 12353, 12354, 12355, 12356, 12357, 12358, 12359, 12360, 12361, 12362, 12363, 12364, 12365, 12366, 12367, 12368, 12369, 12370, 12371, 12372, 12373, 12374, 12375,
  12376, 12377, 12378, 12379, 12380, 12381, 12382, 12383, 12384, 12385, 12386, 12387, 12388, 12389, 12390, 12391, 12392, 12393, 12394, 12395, 12396, 12397, 12398, 12399,
  16384, 16385, 16386, 16387, 16388, 16389, 16390, 16391, 12400, 12401, 12402, 12403, 12404, 12405, 12406, 12407, 12408, 12409, 12410, 12411, 12412, 12413, 12414, 12415,
  16392, 16393, 16394, 16395, 16396, 16397, 16398, 16399, 16400, 16401, 16402, 16403, 16404, 16405, 16406, 16407, 16408, 16409, 16410, 16411, 16412, 16413, 16414, 16415,
};

extern __shared__ __attribute__((aligned(16))) char smem_[];
constexpr unsigned NO_FLAG = 0;
constexpr int NT_FULL = 5 * SEG_LAST, NTOUT = NT_FULL + 10 * (NSTEP - SEG_LAST), NWS = 64;
__global__ __launch_bounds__(512, 2) void k_all(Params P) {
  lptr sm = (lptr)smem_;
  const unsigned hb = hand_base(P);
  Hs hs;
  hs.ws = P.ws; hs.hb = hb; hs.ok = false;
  { unsigned long long t; asm volatile("s_memrealtime %0\n\ts_waitcnt lgkmcnt(0)" : "=s"(t) :: "memory"); hs.clo = (unsigned)t; hs.chi = (unsigned)(t >> 32); }
  gst64(mk_rsrc(P.ws, WS_USED), u2{hs.clo, hs.chi}, threadIdx.x == 0 ? 0 : OOB, hb + HS_C + blockIdx.x * 8);
  const unsigned pfj = (blockIdx.x >> 3) - ((__builtin_amdgcn_s_getreg(20 | (3 << 11)) & 4) ? PFJ_HI : PFJ_LO);
  if (pfj < CODEPF) {
    unsigned long long pc; unsigned len;
    asm volatile("s_getpc_b64 %0\n.Lpf_here:\n\ts_mov_b32 %1, (.Lpf_end-.Lpf_here)&4294967295" : "=s"(pc), "=s"(len));
    len = len < (1u << 20) ? len : 0u;
    const rsrc_t rc = mk_rsrc((const void*)pc, len);
    const unsigned wv = __builtin_amdgcn_readfirstlane(threadIdx.x >> 6);
    for (unsigned o = (pfj * 8 + wv) * 8192; o < len; o += CODEPF * 8 * 8192)
      asm volatile("s_mov_b32 m0, %0\n\ts_nop 0\n\tbuffer_load_dword %1, %2, %3 offen lds" ::"s"((unsigned)(size_t)sm + LDS_BYTES - 1024), "v"(o + (threadIdx.x & 63) * 128), "s"(rc), "s"(0u) : "memory");
  }
  volatile LAS unsigned* sw = (volatile LAS unsigned*)(sm + LDS_SCHED);
  unsigned* const ctr = (unsigned*)(P.ws + hb + CTR_REL);
  hs.rot = ((__builtin_amdgcn_s_getreg(20 | (3 << 11)) & 7) - blockIdx.x) & 7;
  const bool wave0 = __builtin_amdgcn_readfirstlane(threadIdx.x >> 6) == 0;
  if (blockIdx.x == 0) {
    unsigned sent = SENT; asm volatile("" : "+v"(sent));
#pragma unroll
    for (int y = 0; y < 4; ++y) gst128(mk_rsrc(P.ws, WS_USED), u4{sent, sent, sent, sent}, threadIdx.x * 16, hb + CTR_REL + y * 8192);
    WAITV(0); BAR();
    if (wave0) {
      for (int i = 0; i < NECHO; ++i) { hs.echo(threadIdx.x); __builtin_amdgcn_s_sleep(ECHOSL); }
    }
    BAR();
  }
  unsigned sx, sj, role, fold, first;
  bool outw;
  auto assign = [&]() {
    sx = (blockIdx.x + hs.rot) & 7; sj = blockIdx.x >> 3;
    role = 0xFFFFu; fold = 0xFFFFu;
    if (sx < 4) {
      if (sj < 8) role = 8 * sx + sj;
      else if (sj < 28) role = 32 + 16 * ((sj - 8) >> 2) + 4 * sx + ((sj - 8) & 3);
      if (sj >= 24) fold = 64 + 8 * sx + sj - 24;
    } else if (sj < 16) role = NROLE + (sj >> 3) * 32 + 8 * (sx - 4) + (sj & 7);
    else fold = 16 * (sx - 4) + sj - 16;
    first = role;
    outw = sx >= 4 && sj >= 32 - NOUTW;
  };
  assign();
  bool gate = true;
  bool empty = false;
  bool late = false;
  unsigned flag = NO_FLAG;
  for (;;) {
    role = __builtin_amdgcn_readfirstlane(role); fold = __builtin_amdgcn_readfirstlane(fold);
    if (role < (unsigned)(NROLE + NWS) && fold == 0xFFFFu) {
      bool done = true;
      if (role < 32) done = role_x(P, sm, role, hb, hs);
      else if (role < (unsigned)NROLE) done = role_z(P, sm, role - 32, hb, hs);
      else if (role < (unsigned)NROLE + 32) done = role_qk(P, sm, role - NROLE, hb, hs);
      else done = role_vo(P, sm, role - NROLE - 32, hb, hs);
      if (!done) { assign(); continue; }
      role = 0xFFFFu;
    }
    const int tid = TID();
    const rsrc_t rws = mk_rsrc(P.ws, WS_USED);
    unsigned next = fold;
    if (fold == 0xFFFFu) {
      if (tid < 64) {
        unsigned e = 0xFFFFu;
        if (gate && sx == 0 && sj == 31) {
          for (int i = 0; i < POLL_MAX; ++i) {
            if (__builtin_amdgcn_readfirstlane(gld32(rws, 0, hb + CTR_ACK)) + 1u >= 256u) break;
            hs.echo(tid);
            __builtin_amdgcn_s_sleep(8);
          }
        }
        if (gate && sx < 4) poll_words<16>(rws, hb + CTR_START, 1, tid);
        if (gate && first == 0xFFFFu && !outw) wait_until((rt32() + (sx < 4 ? (sj - 28) * 4 + sx : (sj - 16) * 4 + sx - 4) * STAG) & ~3u);
        const unsigned drawn = __builtin_amdgcn_readfirstlane(__hip_atomic_load(ctr + (CTR_TOUT - CTR_REL) / 4, __ATOMIC_RELAXED, __HIP_MEMORY_SCOPE_AGENT) + 1u);
        if (sx < 4 && !late) {
          if (!empty) {
            unsigned head = 0;
            for (int i = 0; i < POLL_MAX; ++i) {
              head = __builtin_amdgcn_readfirstlane(gld32(rws, 0, hb + CTR_AQ)) + 1u;
              if (head >= (unsigned)A2_FIRST) break;
              __builtin_amdgcn_s_sleep(32);
            }
            if (head >= (unsigned)A2_LAST) empty = true;
          }
          if (empty) e = 0x6000u;
        }
        bool take = drawn < (unsigned)NTOUT && (empty || (outw && !gate));
        for (; e == 0xFFFFu;) {
          unsigned t = 0;
          unsigned* const head = ctr + (take ? CTR_TOUT - CTR_REL : CTR_AQ - CTR_REL) / 4;
          if (!take && empty) break;
          if (tid == 0) t = __hip_atomic_fetch_add(head, 1u, __ATOMIC_RELAXED, __HIP_MEMORY_SCOPE_AGENT) + 1u;
          t = __builtin_amdgcn_readfirstlane(t);
          if (take) {
            if (t < (unsigned)NTOUT) { e = 0x5000u | t; break; }
            take = false;
          } else {
            e = t < (unsigned)NQ ? (unsigned)kOrder[t] : 0xFFFFu;
            if (e != 0xFFFFu) break;
            empty = true;
            if (sx < 4 && !late) { e = 0x6000u; break; }
            take = drawn < (unsigned)NTOUT;
          }
        }
        if (tid == 0) { sw[1] = e; sw[2] = empty; }
      }
      gate = false;
      WAITV(0); WAITL(0); BAR();
      if (flag != NO_FLAG && tid == 0) gst32(rws, 1u, 0, flag);
      flag = NO_FLAG;
      next = sw[1];
      empty = __builtin_amdgcn_readfirstlane(sw[2]) != 0;
    }
    fold = 0xFFFFu;
    const unsigned e = __builtin_amdgcn_readfirstlane(next), ty = e >> 12, ix = e & 4095;
    if (e == 0xFFFFu) break;
    const int l = tid & 63, w = __builtin_amdgcn_readfirstlane(tid >> 6);
    if (ty == 0) {
      bool good;
      if (ix < 64 && (ix & 8)) good = task_fold<1>(P, sm, ix, 8 * (ix >> 4) + (ix & 7), 2, hb, hs, false);
      else good = task_fold<2>(P, sm, ix, ix < 64 ? 8 * (ix >> 4) + (ix & 7) : ix - 64, ix < 64 ? 0 : 3, hb, hs, sx == 0 && sj == 31);
      if (!good) assign();
    } else if (ty == 2) {
      task_gemm_rows<3, false>(sm, mk_rsrc(P.Ax, 2048 * 1280), ix / 5, P.Awqkv, 640 * 3840, (ix % 5) * 384, 3840, rws, OFF_QKVA, 3840);
      flag = hb + FL_A1 + ((ix / 5) * 8 + ix % 5) * 4;
    } else if (ty == 3) {
      const int qb = ix >> 3, hg = ix & 7;
      if (w == 0) {
        const int b0 = qb < 2 ? 0 : qb - 2;
        for (int b = b0; b <= qb; ++b) poll_words<16>(rws, hb + FL_A1 + b * 32, 5, l);
      }
      BAR();
      if (qb < 2) task_attn<true>(P, sm, qb, hg); else task_attn<false>(P, sm, qb, hg);
      flag = hb + FL_A2 + ix * 4;
    } else if (ty == 4) {
      if (w == 0) poll_words<16>(rws, hb + FL_A2 + (ix >> 1) * 32, 8, l);
      BAR();
      const rsrc_t rx = mk_rsrc(P.ws + OFF_OA, 2048 * 1280), ro = mk_rsrc(P.out, 2048 * 2560);
      if (ix & 1) task_gemm_rows<2, true>(sm, rx, ix >> 1, P.Awo, 640 * 1280, 384, 1280, ro, 0, 2560);
      else task_gemm_rows<3, true>(sm, rx, ix >> 1, P.Awo, 640 * 1280, 0, 1280, ro, 0, 2560);
    } else if (ty == 6) {
      if (w == 0) (void)poll_above<64>(rws, hb + CTR_PROG, A2DEF);
      BAR();
      late = true;
    } else if (ix >= (unsigned)NT_FULL) {
      const int u = ix - NT_FULL, seg = SEG_LAST + u / 10;
      task_tout_last(P, sm, seg, (u % 10) >> 1, u & 1, [&]() {
        (void)poll_above<16>(rws, hb + CTR_PROG, SEG_LAST - 3);
        (void)poll_progress(rws, hb + PR_VO, 32, 4, seg + 1, l);
      }, [&]() { wait_phase(poll_above<4>(rws, hb + CTR_PROG, SEG_LAST - 1), LASTOFS); },
                     hb + CTR_TOUT, hb + CTR_AQ, NTOUT, NQ, sw + 3, sw + 8);
      if (__builtin_amdgcn_readfirstlane(sw[3]) == 0) break;
    } else {
      const int seg = ix / 5;
      task_tout(P, sm, seg, ix % 5, [&]() { (void)poll_above<16>(rws, hb + CTR_PROG, seg < 2 ? 0 : seg - 2); },
                [&]() { (void)poll_above<PSL>(rws, hb + CTR_PROG, seg); }, hb + CTR_TOUT, hb + CTR_AQ, NTOUT, NQ, sw + 3);
      if (__builtin_amdgcn_readfirstlane(sw[3]) == 0) break;
    }
  }
  asm volatile(".Lpf_end:");
}

extern "C" void kernel_launch(void* const* d_in, const int* in_sizes, int n_in, void* d_out, int out_size, void* d_ws, size_t ws_size, hipStream_t stream) {
  Params P;
  P.Ax = (const char*)d_in[0]; P.Awqkv = (const char*)d_in[1]; P.Awo = (const char*)d_in[2]; P.Bx = (const char*)d_in[3]; P.Bmem0 = (const char*)d_in[4];
  P.Wq = (const char*)d_in[5]; P.Wk = (const char*)d_in[6]; P.Wv = (const char*)d_in[7]; P.Wo = (const char*)d_in[8];
  P.out = (char*)d_out; P.ws = (char*)d_ws;
  (void)hipFuncSetAttribute((const void*)k_all, hipFuncAttributeMaxDynamicSharedMemorySize, LDS_BYTES);
  k_all<<<256, 512, LDS_BYTES, stream>>>(P);
}
```
